```python
import torch

def _softmax(x, axis=-1):
    return torch.softmax(x, dim=axis)
def _gelu(x):
    return torch.nn.functional.gelu(x, approximate="tanh")
def _silu(x):
    return torch.nn.functional.silu(x)
def _sigmoid(x):
    return torch.sigmoid(x)
def _layernorm(x, axis=-1, eps=1e-5):
    m = x.mean(dim=axis, keepdim=True)
    v = x.var(dim=axis, keepdim=True, unbiased=False)
    return (x - m) / torch.sqrt(v + eps)
def _scatter_add(tgt, idx, src, axis=0):
    return tgt.clone().index_add_(axis, idx, src)
def _segment_sum(src, seg, n):
    out = torch.zeros((n,) + src.shape[1:], dtype=src.dtype)
    return out.index_add_(0, seg, src)
def _pad(x, pw):
    flat = tuple(v for pair in reversed(pw) for v in pair)
    return torch.nn.functional.pad(x, flat)

kernel_name = 'interleave_layers__gqa_block_cached__gqa_block_cached__decode'

def setup_inputs():

    _w = (lambda a: (lambda t: t.to(torch.bfloat16) if t.is_floating_point() else t)(torch.as_tensor(a)))
    def _A_setup():
        B, Sq, D = 8, 1, 512
        Hq, Hkv, Sk, Dh = 32, 8, 4096, 128
        assert Hq % Hkv == 0, (Hq, Hkv)
        return {
            "x": _w((lambda *s: torch.randn(*s, dtype=torch.bfloat16) * 0.1)(B, Sq, D)),
            "k_cache": _w((lambda *s: torch.randn(*s, dtype=torch.bfloat16) * 0.8)(B, Hkv, Sk, Dh)),
            "v_cache": _w((lambda *s: torch.randn(*s, dtype=torch.bfloat16) * 3.2)(B, Hkv, Sk, Dh)),
            "wq": _w((lambda *s: torch.randn(*s, dtype=torch.bfloat16) * 0.8)(D, Hq * Dh) * (D ** -0.5)),
            "wo": _w((lambda *s: torch.randn(*s, dtype=torch.bfloat16) * 3.2)(Hq * Dh, D) * ((Hq * Dh) ** -0.5)),
        }
    _A_in = _A_setup()

    def _B_setup():
        B, Sq, D = 8, 1, 512
        Hq, Hkv, Sk, Dh = 32, 8, 4096, 128
        assert Hq % Hkv == 0, (Hq, Hkv)
        return {
            "x": _w((lambda *s: torch.randn(*s, dtype=torch.bfloat16) * 0.1)(B, Sq, D)),
            "k_cache": _w((lambda *s: torch.randn(*s, dtype=torch.bfloat16) * 1.6)(B, Hkv, Sk, Dh)),
            "v_cache": _w((lambda *s: torch.randn(*s, dtype=torch.bfloat16) * 1.6)(B, Hkv, Sk, Dh)),
            "wq": _w((lambda *s: torch.randn(*s, dtype=torch.bfloat16) * 1.6)(D, Hq * Dh) * (D ** -0.5)),
            "wo": _w((lambda *s: torch.randn(*s, dtype=torch.bfloat16) * 1.6)(Hq * Dh, D) * ((Hq * Dh) ** -0.5)),
        }
    _B_in = _B_setup()

    return {
        "A_x": _A_in["x"],
        "A_k_cache": _A_in["k_cache"],
        "A_v_cache": _A_in["v_cache"],
        "A_wq": _A_in["wq"],
        "A_wo": _A_in["wo"],
        "B_x": _B_in["x"],
        "B_k_cache": _B_in["k_cache"],
        "B_v_cache": _B_in["v_cache"],
        "B_wq": _B_in["wq"],
        "B_wo": _B_in["wo"],
    }

def reference(A_x, A_k_cache, A_v_cache, A_wq, A_wo, B_x, B_k_cache, B_v_cache, B_wq, B_wo):
    def _A_ref(x, k_cache, v_cache, wq, wo):
        B, Sq, D = x.shape
        Hq, Dh = 32, 128
        Hkv = k_cache.shape[1]
        rep = Hq // Hkv
        q = (lambda x, *d: x.permute(*d))((x @ wq).reshape(B, Sq, Hq, Dh), 0, 2, 1, 3)
        k = (lambda x, n, axis: torch.repeat_interleave(x, n, dim=axis))(k_cache, rep, axis=1)
        v = (lambda x, n, axis: torch.repeat_interleave(x, n, dim=axis))(v_cache, rep, axis=1)
        scores = torch.einsum("bhqd,bhkd->bhqk", q, k) * (Dh ** -0.5)
        p = _softmax(scores, axis=-1)
        o = torch.einsum("bhqk,bhkd->bhqd", p, v)
        return (lambda x, *d: x.permute(*d))(o, 0, 2, 1, 3).reshape(B, Sq, Hq * Dh) @ wo

    def _B_ref(x, k_cache, v_cache, wq, wo):
        B, Sq, D = x.shape
        Hq, Dh = 32, 128
        Hkv = k_cache.shape[1]
        rep = Hq // Hkv
        q = (lambda x, *d: x.permute(*d))((x @ wq).reshape(B, Sq, Hq, Dh), 0, 2, 1, 3)
        k = (lambda x, n, axis: torch.repeat_interleave(x, n, dim=axis))(k_cache, rep, axis=1)
        v = (lambda x, n, axis: torch.repeat_interleave(x, n, dim=axis))(v_cache, rep, axis=1)
        scores = torch.einsum("bhqd,bhkd->bhqk", q, k) * (Dh ** -0.5)
        p = _softmax(scores, axis=-1)
        o = torch.einsum("bhqk,bhkd->bhqd", p, v)
        return (lambda x, *d: x.permute(*d))(o, 0, 2, 1, 3).reshape(B, Sq, Hq * Dh) @ wo

    x = A_x
    x = x + _A_ref(_layernorm(x, axis=-1), A_k_cache, A_v_cache, A_wq, A_wo)
    x = x + _B_ref(_layernorm(x, axis=-1), B_k_cache, B_v_cache, B_wq, B_wo)
    x = x + _A_ref(_layernorm(x, axis=-1), A_k_cache, A_v_cache, A_wq, A_wo)
    x = x + _B_ref(_layernorm(x, axis=-1), B_k_cache, B_v_cache, B_wq, B_wo)
    return x
```

```cpp
#include <hip/hip_runtime.h>
#include <hip/hip_bf16.h>
#include <stdint.h>
#define KV_NT 0
typedef unsigned v4u __attribute__((ext_vector_type(4)));
typedef float f32x4 __attribute__((ext_vector_type(4)));
typedef short bf16x8 __attribute__((ext_vector_type(8)));
constexpr int NB = 8, DM = 512, HQ = 32, HKV = 8, SK = 4096, DH = 128, NS = 8;

__device__ __forceinline__ float bf2f(unsigned short b) { return __builtin_bit_cast(float, (unsigned)b << 16); }
__device__ __forceinline__ unsigned short f2bf(float f) { __bf16 h = (__bf16)f; return __builtin_bit_cast(unsigned short, h); }
__device__ __forceinline__ unsigned pk2(float lo, float hi) { return (unsigned)f2bf(lo) | ((unsigned)f2bf(hi) << 16); }
__device__ __forceinline__ f32x4 mfma(v4u a, v4u b, f32x4 c) {
  return __builtin_amdgcn_mfma_f32_16x16x32_bf16(__builtin_bit_cast(bf16x8, a), __builtin_bit_cast(bf16x8, b), c, 0, 0, 0);
}
__device__ __forceinline__ v4u ldkv(const void* p) { return KV_NT ? __builtin_nontemporal_load((const v4u*)p) : *(const v4u*)p; }

__global__ void __launch_bounds__(256) k_q(const unsigned short* xin_bf, const float* xin_f, const unsigned short* wq, unsigned short* q) {
  __shared__ float xs[NB][DM];
  __shared__ float red[4][NB][16];
  const int tid = threadIdx.x, lane = tid & 63, w = tid >> 6;
  for (int b = w; b < NB; b += 4) {
    float v[8]; float s = 0.f;
    for (int j = 0; j < 8; ++j) { int c = lane + 64 * j; v[j] = xin_bf ? bf2f(xin_bf[b * DM + c]) : xin_f[b * DM + c]; s += v[j]; }
    for (int o = 32; o; o >>= 1) s += __shfl_xor(s, o);
    float m = s * (1.f / DM); float ss = 0.f;
    for (int j = 0; j < 8; ++j) { float d = v[j] - m; ss += d * d; }
    for (int o = 32; o; o >>= 1) ss += __shfl_xor(ss, o);
    float r = 1.f / sqrtf(ss * (1.f / DM) + 1e-5f);
    for (int j = 0; j < 8; ++j) xs[b][lane + 64 * j] = bf2f(f2bf((v[j] - m) * r));
  }
  __syncthreads();
  const int col = blockIdx.x * 16 + (tid & 15), kg = tid >> 4;
  float acc[NB];
  for (int b = 0; b < NB; ++b) acc[b] = 0.f;
  for (int k = kg * 32; k < kg * 32 + 32; ++k) {
    float wv = bf2f(wq[(size_t)k * (HQ * DH) + col]);
    for (int b = 0; b < NB; ++b) acc[b] += xs[b][k] * wv;
  }
  for (int b = 0; b < NB; ++b) { acc[b] += __shfl_xor(acc[b], 16); acc[b] += __shfl_xor(acc[b], 32); }
  if (lane < 16) for (int b = 0; b < NB; ++b) red[w][b][lane] = acc[b];
  __syncthreads();
  if (tid < NB * 16) { int b = tid >> 4, c = tid & 15; float s = red[0][b][c] + red[1][b][c] + red[2][b][c] + red[3][b][c]; q[b * (HQ * DH) + blockIdx.x * 16 + c] = f2bf(s); }
}

template <int C> __device__ __forceinline__ float dpp(float x) {
  return __builtin_bit_cast(float, __builtin_amdgcn_mov_dpp(__builtin_bit_cast(int, x), C, 0xf, 0xf, true));
}

__global__ void __launch_bounds__(256) k_attn(const unsigned short* q, const unsigned short* kc, const unsigned short* vc, float* po, float* pml) {
  __shared__ float mrg[4][4][DH + 4];
  const int tid = threadIdx.x, lane = tid & 63, w = tid >> 6, g = lane >> 4, r = lane & 15;
  const int pair = blockIdx.y, b = pair >> 3, hk = pair & 7, sp = blockIdx.x;
  const int kb = sp * (SK / NS) + w * 128;
  const char* kbase = (const char*)(kc + ((size_t)pair * SK + kb) * DH);
  const char* vbase = (const char*)(vc + ((size_t)pair * SK + kb) * DH);
  v4u kf[8][4], vr[4][8];
#pragma unroll
  for (int t = 0; t < 8; ++t)
#pragma unroll
    for (int i = 0; i < 4; ++i) kf[t][i] = ldkv(kbase + (16 * t + 4 * (r & 3) + (r >> 2)) * 256 + i * 64 + g * 16);
  v4u qf[4];
#pragma unroll
  for (int i = 0; i < 4; ++i) qf[i] = *(const v4u*)(q + (size_t)b * (HQ * DH) + (hk * 4 + (r & 3)) * DH + 32 * i + 8 * g);
  f32x4 S[8];
#pragma unroll
  for (int t = 0; t < 8; ++t) { S[t] = f32x4{0, 0, 0, 0};
#pragma unroll
    for (int i = 0; i < 4; ++i) S[t] = mfma(kf[t][i], qf[i], S[t]); }
#pragma unroll
  for (int s = 0; s < 4; ++s)
#pragma unroll
    for (int e = 0; e < 8; ++e) vr[s][e] = ldkv(vbase + s * 8192 + e * 1024 + lane * 16);
  const int sl = r >> 2;
  float sv[8];
#pragma unroll
  for (int e = 0; e < 8; ++e) {
    float a0 = S[0 + (e >> 2)][e & 3], a1 = S[2 + (e >> 2)][e & 3], a2 = S[4 + (e >> 2)][e & 3], a3 = S[6 + (e >> 2)][e & 3];
    sv[e] = (sl == 0 ? a0 : sl == 1 ? a1 : sl == 2 ? a2 : a3) * (0.08838834764831845f * 1.4426950408889634f);
  }
  float m = sv[0];
#pragma unroll
  for (int e = 1; e < 8; ++e) m = fmaxf(m, sv[e]);
  m = fmaxf(m, __shfl_xor(m, 4)); m = fmaxf(m, __shfl_xor(m, 8)); m = fmaxf(m, __shfl_xor(m, 16)); m = fmaxf(m, __shfl_xor(m, 32));
  float l = 0.f; float p[8];
#pragma unroll
  for (int e = 0; e < 8; ++e) { p[e] = __builtin_amdgcn_exp2f(sv[e] - m); l += p[e]; }
  l += __shfl_xor(l, 4); l += __shfl_xor(l, 8); l += __shfl_xor(l, 16); l += __shfl_xor(l, 32);
  v4u pb = {pk2(p[0], p[1]), pk2(p[2], p[3]), pk2(p[4], p[5]), pk2(p[6], p[7])};
  f32x4 acc[8];
#pragma unroll
  for (int j = 0; j < 8; ++j) acc[j] = f32x4{0, 0, 0, 0};
#pragma unroll
  for (int s = 0; s < 4; ++s) {
    v4u pm = (sl == s) ? pb : v4u{0, 0, 0, 0};
#pragma unroll
    for (int j = 0; j < 8; ++j) {
      v4u a;
#pragma unroll
      for (int pp = 0; pp < 4; ++pp) a[pp] = __builtin_amdgcn_perm(vr[s][2 * pp + 1][j >> 1], vr[s][2 * pp][j >> 1], (j & 1) ? 0x07060302u : 0x05040100u);
      acc[j] = mfma(a, pm, acc[j]);
    }
  }
#pragma unroll
  for (int j = 0; j < 8; ++j)
#pragma unroll
    for (int jj = 0; jj < 4; ++jj) { float v = acc[j][jj]; v += __shfl_xor(v, 4); v += __shfl_xor(v, 8); acc[j][jj] = v; }
  if (sl == 0) {
    const int h = r & 3;
#pragma unroll
    for (int j = 0; j < 8; ++j)
#pragma unroll
      for (int jj = 0; jj < 4; ++jj) mrg[w][h][32 * g + 8 * jj + j] = acc[j][jj];
    if (g == 0) { mrg[w][h][DH] = m; mrg[w][h][DH + 1] = l; }
  }
  __syncthreads();
  {
    const int h = w;
    float M = fmaxf(fmaxf(mrg[0][h][DH], mrg[1][h][DH]), fmaxf(mrg[2][h][DH], mrg[3][h][DH]));
    float L = 0.f, o0 = 0.f, o1 = 0.f;
    for (int ww = 0; ww < 4; ++ww) { float wt = __builtin_amdgcn_exp2f(mrg[ww][h][DH] - M); L += wt * mrg[ww][h][DH + 1]; o0 += wt * mrg[ww][h][2 * lane]; o1 += wt * mrg[ww][h][2 * lane + 1]; }
    size_t row = ((size_t)b * HQ + hk * 4 + h) * NS + sp;
    po[row * DH + 2 * lane] = o0; po[row * DH + 2 * lane + 1] = o1;
    if (lane == 0) { pml[row * 2] = M; pml[row * 2 + 1] = L; }
  }
}

__global__ void __launch_bounds__(256) k_o(const float* po, const float* pml, const unsigned short* wo, float* ypart) {
  __shared__ float os[NB][DH];
  __shared__ float red[2][NB][128];
  const int tid = threadIdx.x, h = blockIdx.x >> 2, cg = blockIdx.x & 3;
  for (int i = tid; i < NB * DH; i += 256) {
    int b = i >> 7, d = i & 127; size_t row = ((size_t)b * HQ + h) * NS;
    float M = -INFINITY; for (int s = 0; s < NS; ++s) M = fmaxf(M, pml[(row + s) * 2]);
    float L = 0.f, o = 0.f;
    for (int s = 0; s < NS; ++s) { float wt = __builtin_amdgcn_exp2f(pml[(row + s) * 2] - M); L += wt * pml[(row + s) * 2 + 1]; o += wt * po[(row + s) * DH + d]; }
    os[b][d] = bf2f(f2bf(o / L));
  }
  __syncthreads();
  const int c = tid & 127, kh = tid >> 7;
  float acc[NB]; for (int b = 0; b < NB; ++b) acc[b] = 0.f;
  for (int k = kh * 64; k < kh * 64 + 64; ++k) { float wv = bf2f(wo[(size_t)(h * DH + k) * DM + cg * 128 + c]); for (int b = 0; b < NB; ++b) acc[b] += os[b][k] * wv; }
  for (int b = 0; b < NB; ++b) red[kh][b][c] = acc[b];
  __syncthreads();
  for (int i = tid; i < NB * 128; i += 256) { int b = i >> 7, cc = i & 127; ypart[((size_t)h * NB + b) * DM + cg * 128 + cc] = red[0][b][cc] + red[1][b][cc]; }
}

__global__ void __launch_bounds__(256) k_res(const unsigned short* xin_bf, const float* xin_f, const float* ypart, float* xout, unsigned short* out_bf) {
  int i = blockIdx.x * 256 + threadIdx.x;
  float s = 0.f; for (int h = 0; h < HQ; ++h) s += ypart[(size_t)h * NB * DM + i];
  float x = xin_bf ? bf2f(xin_bf[i]) : xin_f[i];
  float r = bf2f(f2bf(x + bf2f(f2bf(s))));
  xout[i] = r; if (out_bf) out_bf[i] = f2bf(r);
}

extern "C" void kernel_launch(void* const* d_in, const int* in_sizes, int n_in, void* d_out, int out_size, void* d_ws, size_t ws_size, hipStream_t stream) {
  const unsigned short* x0 = (const unsigned short*)d_in[0];
  char* ws = (char*)d_ws;
  unsigned short* q = (unsigned short*)ws;
  float* xa = (float*)(ws + 65536);
  float* po = (float*)(ws + 131072);
  float* pml = (float*)(ws + 131072 + 1048576);
  float* ypart = (float*)(ws + 131072 + 1048576 + 65536);
  for (int L = 0; L < 4; ++L) {
    const int o = (L & 1) ? 5 : 0;
    const float* xf = xa + ((L + 1) & 1) * 4096; float* xo = xa + (L & 1) * 4096;
    k_q<<<256, 256, 0, stream>>>(L == 0 ? x0 : nullptr, xf, (const unsigned short*)d_in[o + 3], q);
    k_attn<<<dim3(NS, 64), 256, 0, stream>>>(q, (const unsigned short*)d_in[o + 1], (const unsigned short*)d_in[o + 2], po, pml);
    k_o<<<128, 256, 0, stream>>>(po, pml, (const unsigned short*)d_in[o + 4], ypart);
    k_res<<<16, 256, 0, stream>>>(L == 0 ? x0 : nullptr, xf, ypart, xo, L == 3 ? (unsigned short*)d_out : nullptr);
  }
}
```

```cpp
#include <hip/hip_runtime.h>
#include <hip/hip_bf16.h>
#include <stdint.h>
#define KV_NT 1
typedef unsigned v4u __attribute__((ext_vector_type(4)));
typedef float f32x4 __attribute__((ext_vector_type(4)));
typedef short bf16x8 __attribute__((ext_vector_type(8)));
constexpr int NB = 8, DM = 512, HQ = 32, HKV = 8, SK = 4096, DH = 128, NS = 8;

__device__ __forceinline__ float bf2f(unsigned short b) { return __builtin_bit_cast(float, (unsigned)b << 16); }
__device__ __forceinline__ unsigned short f2bf(float f) { __bf16 h = (__bf16)f; return __builtin_bit_cast(unsigned short, h); }
__device__ __forceinline__ unsigned pk2(float lo, float hi) { return (unsigned)f2bf(lo) | ((unsigned)f2bf(hi) << 16); }
__device__ __forceinline__ f32x4 mfma(v4u a, v4u b, f32x4 c) {
  return __builtin_amdgcn_mfma_f32_16x16x32_bf16(__builtin_bit_cast(bf16x8, a), __builtin_bit_cast(bf16x8, b), c, 0, 0, 0);
}
__device__ __forceinline__ v4u ldkv(const void* p) { return KV_NT ? __builtin_nontemporal_load((const v4u*)p) : *(const v4u*)p; }

__global__ void __launch_bounds__(256) k_q(const unsigned short* xin_bf, const float* xin_f, const unsigned short* wq, unsigned short* q) {
  __shared__ float xs[NB][DM];
  __shared__ float red[4][NB][16];
  const int tid = threadIdx.x, lane = tid & 63, w = tid >> 6;
  for (int b = w; b < NB; b += 4) {
    float v[8]; float s = 0.f;
    for (int j = 0; j < 8; ++j) { int c = lane + 64 * j; v[j] = xin_bf ? bf2f(xin_bf[b * DM + c]) : xin_f[b * DM + c]; s += v[j]; }
    for (int o = 32; o; o >>= 1) s += __shfl_xor(s, o);
    float m = s * (1.f / DM); float ss = 0.f;
    for (int j = 0; j < 8; ++j) { float d = v[j] - m; ss += d * d; }
    for (int o = 32; o; o >>= 1) ss += __shfl_xor(ss, o);
    float r = 1.f / sqrtf(ss * (1.f / DM) + 1e-5f);
    for (int j = 0; j < 8; ++j) xs[b][lane + 64 * j] = bf2f(f2bf((v[j] - m) * r));
  }
  __syncthreads();
  const int col = blockIdx.x * 16 + (tid & 15), kg = tid >> 4;
  float acc[NB];
  for (int b = 0; b < NB; ++b) acc[b] = 0.f;
  for (int k = kg * 32; k < kg * 32 + 32; ++k) {
    float wv = bf2f(wq[(size_t)k * (HQ * DH) + col]);
    for (int b = 0; b < NB; ++b) acc[b] += xs[b][k] * wv;
  }
  for (int b = 0; b < NB; ++b) { acc[b] += __shfl_xor(acc[b], 16); acc[b] += __shfl_xor(acc[b], 32); }
  if (lane < 16) for (int b = 0; b < NB; ++b) red[w][b][lane] = acc[b];
  __syncthreads();
  if (tid < NB * 16) { int b = tid >> 4, c = tid & 15; float s = red[0][b][c] + red[1][b][c] + red[2][b][c] + red[3][b][c]; q[b * (HQ * DH) + blockIdx.x * 16 + c] = f2bf(s); }
}

template <int C> __device__ __forceinline__ float dpp(float x) {
  return __builtin_bit_cast(float, __builtin_amdgcn_mov_dpp(__builtin_bit_cast(int, x), C, 0xf, 0xf, true));
}

__global__ void __launch_bounds__(256) k_attn(const unsigned short* q, const unsigned short* kc, const unsigned short* vc, float* po, float* pml) {
  __shared__ float mrg[4][4][DH + 4];
  const int tid = threadIdx.x, lane = tid & 63, w = tid >> 6, g = lane >> 4, r = lane & 15;
  const int pair = blockIdx.y, b = pair >> 3, hk = pair & 7, sp = blockIdx.x;
  const int kb = sp * (SK / NS) + w * 128;
  const char* kbase = (const char*)(kc + ((size_t)pair * SK + kb) * DH);
  const char* vbase = (const char*)(vc + ((size_t)pair * SK + kb) * DH);
  v4u kf[8][4], vr[4][8];
#pragma unroll
  for (int t = 0; t < 8; ++t)
#pragma unroll
    for (int i = 0; i < 4; ++i) kf[t][i] = ldkv(kbase + (16 * t + 4 * (r & 3) + (r >> 2)) * 256 + i * 64 + g * 16);
  v4u qf[4];
#pragma unroll
  for (int i = 0; i < 4; ++i) qf[i] = *(const v4u*)(q + (size_t)b * (HQ * DH) + (hk * 4 + (r & 3)) * DH + 32 * i + 8 * g);
  f32x4 S[8];
#pragma unroll
  for (int t = 0; t < 8; ++t) { S[t] = f32x4{0, 0, 0, 0};
#pragma unroll
    for (int i = 0; i < 4; ++i) S[t] = mfma(kf[t][i], qf[i], S[t]); }
#pragma unroll
  for (int s = 0; s < 4; ++s)
#pragma unroll
    for (int e = 0; e < 8; ++e) vr[s][e] = ldkv(vbase + s * 8192 + e * 1024 + lane * 16);
  const int sl = r >> 2;
  float sv[8];
#pragma unroll
  for (int e = 0; e < 8; ++e) {
    float a0 = S[0 + (e >> 2)][e & 3], a1 = S[2 + (e >> 2)][e & 3], a2 = S[4 + (e >> 2)][e & 3], a3 = S[6 + (e >> 2)][e & 3];
    sv[e] = (sl == 0 ? a0 : sl == 1 ? a1 : sl == 2 ? a2 : a3) * (0.08838834764831845f * 1.4426950408889634f);
  }
  float m = sv[0];
#pragma unroll
  for (int e = 1; e < 8; ++e) m = fmaxf(m, sv[e]);
  m = fmaxf(m, __shfl_xor(m, 4)); m = fmaxf(m, __shfl_xor(m, 8)); m = fmaxf(m, __shfl_xor(m, 16)); m = fmaxf(m, __shfl_xor(m, 32));
  float l = 0.f; float p[8];
#pragma unroll
  for (int e = 0; e < 8; ++e) { p[e] = __builtin_amdgcn_exp2f(sv[e] - m); l += p[e]; }
  l += __shfl_xor(l, 4); l += __shfl_xor(l, 8); l += __shfl_xor(l, 16); l += __shfl_xor(l, 32);
  v4u pb = {pk2(p[0], p[1]), pk2(p[2], p[3]), pk2(p[4], p[5]), pk2(p[6], p[7])};
  f32x4 acc[8];
#pragma unroll
  for (int j = 0; j < 8; ++j) acc[j] = f32x4{0, 0, 0, 0};
#pragma unroll
  for (int s = 0; s < 4; ++s) {
    v4u pm = (sl == s) ? pb : v4u{0, 0, 0, 0};
#pragma unroll
    for (int j = 0; j < 8; ++j) {
      v4u a;
#pragma unroll
      for (int pp = 0; pp < 4; ++pp) a[pp] = __builtin_amdgcn_perm(vr[s][2 * pp + 1][j >> 1], vr[s][2 * pp][j >> 1], (j & 1) ? 0x07060302u : 0x05040100u);
      acc[j] = mfma(a, pm, acc[j]);
    }
  }
#pragma unroll
  for (int j = 0; j < 8; ++j)
#pragma unroll
    for (int jj = 0; jj < 4; ++jj) { float v = acc[j][jj]; v += __shfl_xor(v, 4); v += __shfl_xor(v, 8); acc[j][jj] = v; }
  if (sl == 0) {
    const int h = r & 3;
#pragma unroll
    for (int j = 0; j < 8; ++j)
#pragma unroll
      for (int jj = 0; jj < 4; ++jj) mrg[w][h][32 * g + 8 * jj + j] = acc[j][jj];
    if (g == 0) { mrg[w][h][DH] = m; mrg[w][h][DH + 1] = l; }
  }
  __syncthreads();
  {
    const int h = w;
    float M = fmaxf(fmaxf(mrg[0][h][DH], mrg[1][h][DH]), fmaxf(mrg[2][h][DH], mrg[3][h][DH]));
    float L = 0.f, o0 = 0.f, o1 = 0.f;
    for (int ww = 0; ww < 4; ++ww) { float wt = __builtin_amdgcn_exp2f(mrg[ww][h][DH] - M); L += wt * mrg[ww][h][DH + 1]; o0 += wt * mrg[ww][h][2 * lane]; o1 += wt * mrg[ww][h][2 * lane + 1]; }
    size_t row = ((size_t)b * HQ + hk * 4 + h) * NS + sp;
    po[row * DH + 2 * lane] = o0; po[row * DH + 2 * lane + 1] = o1;
    if (lane == 0) { pml[row * 2] = M; pml[row * 2 + 1] = L; }
  }
}

__global__ void __launch_bounds__(256) k_o(const float* po, const float* pml, const unsigned short* wo, float* ypart) {
  __shared__ float os[NB][DH];
  __shared__ float red[2][NB][128];
  const int tid = threadIdx.x, h = blockIdx.x >> 2, cg = blockIdx.x & 3;
  for (int i = tid; i < NB * DH; i += 256) {
    int b = i >> 7, d = i & 127; size_t row = ((size_t)b * HQ + h) * NS;
    float M = -INFINITY; for (int s = 0; s < NS; ++s) M = fmaxf(M, pml[(row + s) * 2]);
    float L = 0.f, o = 0.f;
    for (int s = 0; s < NS; ++s) { float wt = __builtin_amdgcn_exp2f(pml[(row + s) * 2] - M); L += wt * pml[(row + s) * 2 + 1]; o += wt * po[(row + s) * DH + d]; }
    os[b][d] = bf2f(f2bf(o / L));
  }
  __syncthreads();
  const int c = tid & 127, kh = tid >> 7;
  float acc[NB]; for (int b = 0; b < NB; ++b) acc[b] = 0.f;
  for (int k = kh * 64; k < kh * 64 + 64; ++k) { float wv = bf2f(wo[(size_t)(h * DH + k) * DM + cg * 128 + c]); for (int b = 0; b < NB; ++b) acc[b] += os[b][k] * wv; }
  for (int b = 0; b < NB; ++b) red[kh][b][c] = acc[b];
  __syncthreads();
  for (int i = tid; i < NB * 128; i += 256) { int b = i >> 7, cc = i & 127; ypart[((size_t)h * NB + b) * DM + cg * 128 + cc] = red[0][b][cc] + red[1][b][cc]; }
}

__global__ void __launch_bounds__(256) k_res(const unsigned short* xin_bf, const float* xin_f, const float* ypart, float* xout, unsigned short* out_bf) {
  int i = blockIdx.x * 256 + threadIdx.x;
  float s = 0.f; for (int h = 0; h < HQ; ++h) s += ypart[(size_t)h * NB * DM + i];
  float x = xin_bf ? bf2f(xin_bf[i]) : xin_f[i];
  float r = bf2f(f2bf(x + bf2f(f2bf(s))));
  xout[i] = r; if (out_bf) out_bf[i] = f2bf(r);
}

template<int NT>
__global__ void __launch_bounds__(256) k_stream(const char* k0, const char* v0, const char* k1, const char* v1, unsigned* sink, int ntens, unsigned long long* stamps) {
  const int lane = threadIdx.x & 63, w = threadIdx.x >> 6;
  v4u acc = {0,0,0,0};
  unsigned long long t0 = __builtin_amdgcn_s_memrealtime();
  if (threadIdx.x == 0) stamps[blockIdx.x * 8] = t0;
  for (int rep = 0; rep < 3; ++rep) {
  for (int t = 0; t < ntens; ++t) {
    const char* base = (t == 0 ? k0 : t == 1 ? v0 : t == 2 ? k1 : v1) + (size_t)blockIdx.x * 262144;
    for (int c = w; c < 32; c += 4) {
      v4u r[8];
#pragma unroll
      for (int u = 0; u < 8; ++u) { const v4u* a = (const v4u*)(base + c * 8192 + u * 1024 + lane * 16); r[u] = NT ? __builtin_nontemporal_load(a) : *a; }
      __builtin_amdgcn_sched_barrier(0);
#pragma unroll
      for (int u = 0; u < 8; ++u) acc ^= r[u];
    }
  }
  __syncthreads();
  if (threadIdx.x == 0) stamps[blockIdx.x * 8 + 1 + rep] = __builtin_amdgcn_s_memrealtime();
  }
  if ((acc.x ^ acc.y ^ acc.z ^ acc.w) == 0x12345678u && acc.x == 0x9abcdef0u) sink[0] = 1;
}
__global__ void k_spin(const unsigned long long* stamps, int p) {
  unsigned long long a = (p == 0) ? ~0ull : 0ull, b = 0;
  for (int i = 0; i < 256; ++i) { unsigned long long s = stamps[i * 8 + p], e = stamps[i * 8 + p + 1]; if (p == 0) a = s < a ? s : a; else a = s > a ? s : a; b = e > b ? e : b; }
  unsigned long long d = (b - a) * 20; if (d > 200000) d = 200000;
  unsigned long long t0 = __builtin_amdgcn_s_memrealtime();
  while (__builtin_amdgcn_s_memrealtime() - t0 < d) __builtin_amdgcn_s_sleep(1);
}
extern "C" void kernel_launch(void* const* d_in, const int* in_sizes, int n_in, void* d_out, int out_size, void* d_ws, size_t ws_size, hipStream_t stream) {
  const unsigned short* x0 = (const unsigned short*)d_in[0];
  char* ws = (char*)d_ws;
  unsigned short* q = (unsigned short*)ws;
  float* xa = (float*)(ws + 65536);
  float* po = (float*)(ws + 131072);
  float* pml = (float*)(ws + 131072 + 1048576);
  float* ypart = (float*)(ws + 131072 + 1048576 + 65536);
  k_stream<0><<<256,256,0,stream>>>((const char*)d_in[1], (const char*)d_in[2], (const char*)d_in[6], (const char*)d_in[7], (unsigned*)(ws + (64<<20)), 2, (unsigned long long*)(ws + (65<<20)));
  k_spin<<<1,1,0,stream>>>((const unsigned long long*)(ws + (65<<20)), 2);
  for (int L = 0; L < 4; ++L) {
    const int o = (L & 1) ? 5 : 0;
    const float* xf = xa + ((L + 1) & 1) * 4096; float* xo = xa + (L & 1) * 4096;
    k_q<<<256, 256, 0, stream>>>(L == 0 ? x0 : nullptr, xf, (const unsigned short*)d_in[o + 3], q);
    k_attn<<<dim3(NS, 64), 256, 0, stream>>>(q, (const unsigned short*)d_in[o + 1], (const unsigned short*)d_in[o + 2], po, pml);
    k_o<<<128, 256, 0, stream>>>(po, pml, (const unsigned short*)d_in[o + 4], ypart);
    k_res<<<16, 256, 0, stream>>>(L == 0 ? x0 : nullptr, xf, ypart, xo, L == 3 ? (unsigned short*)d_out : nullptr);
  }
}
```

```cpp
#include <hip/hip_runtime.h>
#include <stdint.h>
#include <stddef.h>

typedef unsigned v4u __attribute__((ext_vector_type(4)));
typedef unsigned v2u __attribute__((ext_vector_type(2)));
typedef __amdgpu_buffer_rsrc_t rsrc_t;
#define LAS __attribute__((address_space(3)))

#define GTICK 76
#define SLACK 4
#define EPS 12
#define TUNE 0
#define STAMPS 0
static unsigned g_tune[18] = {1135,1725,165,1060,1040,1335,570,550,1200,1000,500,1100,800,990,995,1040,200,450};

constexpr unsigned LDS_WAVE = 36864, SCR = 147456;
constexpr unsigned REC = SCR, REC_W = 2112, REC_H = 528;
constexpr unsigned R1 = SCR + 8448, R2 = R1 + 2048;
constexpr unsigned XS = R2 + 1600;
constexpr unsigned TAGW = XS + 4096 + 184;
constexpr unsigned LDS_TOTAL = 163840;
static_assert(XS + 4096 + 192 <= LDS_TOTAL, "lds");
constexpr unsigned WS_E = 0, WS_RDY = 4096, WS_SLOT = 65536 + 256;
constexpr unsigned SLOT_SZ = 1792, CU_SZ = 14592, O_LSE = 1024, O_Y = 1536, O_Q = 1664, O_Q0 = 0u - 256u;
constexpr unsigned WS_END = WS_SLOT - 256 + 256 * CU_SZ;
constexpr unsigned WS_STAMP = WS_END;
constexpr unsigned NOWHERE = 0x7f000000u;
constexpr unsigned SENT = 0xFFFFFFFFu;
constexpr int SPIN_CAP = 1 << 14;
constexpr int AUX_SC1 = 16;

#define STR2(x) #x
#define STR1(x) STR2(x)
#define TP(n)
#define TPCLOB
#define STAMP2_STR
#define ST_STR(k)
#define STAMP(i) do { } while (0)

#define XSTOP_STR
static_assert(XS + 4096 == 163648, "stamp area");

__device__ __forceinline__ void main_loop(unsigned long long ka, rsrc_t rws, unsigned w, unsigned ldsw, unsigned mine, unsigned clus, unsigned qb, unsigned kv0, unsigned wo_off, unsigned wq_off, unsigned ybase, unsigned outw, unsigned fin, unsigned tp0) {
  asm volatile(
    "s_mov_b32 s81, 0\n\t"
    "s_mov_b32 s38, %[tp0]\n\t"
    "s_load_dword s39, %[ka], 152\n\t"
    "s_mul_i32 s26, %[w], 2112\n\t"
    "s_add_u32 s26, s26, 147456\n\t"
    "s_mul_i32 s27, %[w], 528\n\t"
    "s_add_u32 s27, s27, 147456\n\t"
    "s_mov_b32 s28, 0x3e0293ee\n\t"
    "s_mov_b32 s77, 155904\n\t"
    "s_lshl_b32 s45, %[w], 9\n\t"
    "s_add_u32 s45, s45, s77\n\t"
    "s_mov_b32 s54, 0x4000000\n\t"
    "s_mov_b32 s55, 0x20000\n\t"
    "s_mov_b32 s58, 0x4000000\n\t"
    "s_mov_b32 s59, 0x20000\n\t"
    "s_mov_b32 s62, 0x400000\n\t"
    "s_mov_b32 s63, 0x20000\n\t"
    "s_mov_b32 s66, 0x4000000\n\t"
    "s_mov_b32 s67, 0x20000\n\t"
    "s_mov_b32 s70, 0x400000\n\t"
    "s_mov_b32 s71, 0x20000\n\t"
    "s_cmp_gt_u32 s81, 5\n\t"
    "s_cbranch_scc1 92f\n\t"
    "s_and_b32 s86, s81, 1\n\t"
    "s_lshl_b32 s86, s86, 6\n\t"
    "s_bitcmp1_b32 %[outw], 11\n\t"
    "s_cselect_b32 s87, 96, 0\n\t"
    "s_lshl4_add_u32 s87, s81, s87\n\t"
    "s_load_dwordx2 s[52:53], %[ka], s86 offset:0\n\t"
    "s_load_dwordx2 s[56:57], %[ka], s86 offset:8\n\t"
    "s_load_dwordx2 s[60:61], %[ka], s86 offset:16\n\t"
    "s_load_dwordx2 s[64:65], %[ka], s86 offset:32\n\t"
    "s_load_dwordx2 s[72:73], %[ka], s87 offset:160\n\t"
    "s_mul_i32 s86, s81, 6\n\t"
    "s_mov_b32 s88, 0xc23d6d74\n\t"
    "s_lshr_b32 s25, s88, s86\n\t"
    "s_lshr_b32 s86, s81, 2\n\t"
    "s_lshl1_add_u32 s86, s86, s81\n\t"
    "s_mul_i32 s86, s86, 1792\n\t"
    "s_add_u32 s86, s86, %[qb]\n\t"
    "s_add_u32 s29, s86, -128\n\t"
    "s_add_u32 s87, s81, 2\n\t"
    "s_lshr_b32 s87, s87, 2\n\t"
    "s_lshl_b32 s86, s87, 25\n\t"
    "s_add_u32 s35, %[kv0], s86\n\t"
    "s_lshl1_add_u32 s87, s87, s81\n\t"
    "s_mul_i32 s87, s87, 1792\n\t"
    "s_add_u32 s86, s87, -128\n\t"
    "s_lshl_b32 s88, s81, 6\n\t"
    "s_sub_u32 s88, s88, 256\n\t"
    "s_and_b32 s89, s81, 5\n\t"
    "s_cselect_b32 s86, s86, s88\n\t"
    "s_add_u32 s30, s86, %[qb]\n\t"
    "s_add_u32 s87, s87, %[mine]\n\t"
    "s_lshl_b32 s86, %[w], 7\n\t"
    "s_lshl1_add_u32 s31, s86, s87\n\t"
    "s_add_u32 s87, s87, s86\n\t"
    "s_add_u32 s32, s87, 1024\n\t"
    "s_mov_b32 s86, -3584\n\t"
    "s_cmp_lt_u32 s81, 2\n\t"
    "s_cselect_b32 s86, 0x7f000000, s86\n\t"
    "s_add_u32 s33, s31, s86\n\t"
    "s_add_u32 s34, s32, s86\n\t"
    "s_min_u32 s86, s81, 1\n\t"
    "s_lshl_b32 s86, s86, 25\n\t"
    "s_add_u32 s36, %[kv0], s86\n\t"
    "s_lshl_b32 s86, s81, 3\n\t"
    "s_and_b32 s87, %[w], 2\n\t"
    "s_lshl1_add_u32 s86, s87, s86\n\t"
    "s_lshr_b32 s86, 0x765342, s86\n\t"
    "s_and_b32 s86, s86, 7\n\t"
    "s_mul_i32 s86, s86, 1792\n\t"
    "s_and_b32 s87, %[w], 1\n\t"
    "s_lshl_b32 s87, s87, 10\n\t"
    "s_add_u32 s86, s86, s87\n\t"
    "s_add_u32 s86, s86, %[mine]\n\t"
    "s_cmp_lt_u32 s81, 3\n\t"
    "s_cselect_b32 s37, s86, 0x7f000000\n\t"
    "92:\n\t"
    "s_waitcnt lgkmcnt(0)\n\t"
    ".Lgqa_step:\n\t"
    "s_mov_b32 s24, s81\n\t"
    TP(0)
    "v_mbcnt_lo_u32_b32 v0, -1, 0\n\t"
    "v_mbcnt_hi_u32_b32 v0, -1, v0\n\t"
    "s_bitcmp1_b32 s25, 1\n\t"
    "s_cbranch_scc0 14f\n\t"
    "v_and_b32 v1, 3, v0\n\t"
    "v_lshrrev_b32 v2, 4, v0\n\t"
    "v_lshl_add_u32 v1, v1, 2, v2\n\t"
    "v_mul_u32_u24 v1, 14592, v1\n\t"
    "v_and_b32 v2, 12, v0\n\t"
    "v_lshl_add_u32 v1, v2, 2, v1\n\t"
    "s_mov_b32 s92, 0\n\t"
    "v_and_b32 v5, 3, v0\n\t"
    "v_mul_u32_u24 v5, 528, v5\n\t"
    "v_add_u32 v5, s26, v5\n\t"
    "v_and_b32 v2, 12, v0\n\t"
    "v_lshl_add_u32 v4, v2, 4, v5\n\t"
    "v_and_b32 v2, 48, v0\n\t"
    "v_add_u32 v4, v4, v2\n\t"
    "v_lshl_add_u32 v28, v0, 3, s27\n\t"
    "v_lshlrev_b32 v30, 24, v0\n\t"
    "v_lshlrev_b32 v29, 2, v0\n\t"
    "v_lshrrev_b32 v2, 2, v0\n\t"
    "v_lshlrev_b32 v31, 8, v2\n\t"
    "v_and_b32 v2, 7, v2\n\t"
    "v_lshl_add_u32 v31, v2, 5, v31\n\t"
    "v_and_b32 v2, 3, v0\n\t"
    "v_lshl_add_u32 v31, v2, 3, v31\n\t"
    "v_add_u32 v31, %[ldsw], v31\n\t"
    "s_mov_b32 s84, 0xf000f0\n\t"
    "s_mov_b32 s85, 0xf000f0\n\t"
    "s_mov_b32 s86, 0xf000f00\n\t"
    "s_mov_b32 s87, 0xf000f00\n\t"
    "s_mov_b32 s88, 0xf000f000\n\t"
    "s_mov_b32 s89, 0xf000f000\n\t"
    "s_bitcmp1_b32 s25, 0\n\t"
    "s_cselect_b32 s91, 8192, 0\n\t"
    "v_add_u32 v31, s91, v31\n\t"
    "v_xor_b32 v2, 96, v31\n\t"
    "ds_read_b64_tr_b16 v[44:45], v2 offset:20480\n\t"
    "ds_read_b64_tr_b16 v[46:47], v2 offset:24576\n\t"
    "v_xor_b32 v3, 128, v31\n\t"
    "ds_read_b64_tr_b16 v[48:49], v3 offset:20480\n\t"
    "ds_read_b64_tr_b16 v[50:51], v3 offset:24576\n\t"
    "v_xor_b32 v2, 160, v31\n\t"
    "ds_read_b64_tr_b16 v[52:53], v2 offset:20480\n\t"
    "ds_read_b64_tr_b16 v[54:55], v2 offset:24576\n\t"
    "v_xor_b32 v3, 224, v31\n\t"
    "ds_read_b64_tr_b16 v[24:25], v3 offset:20480\n\t"
    "ds_read_b64_tr_b16 v[26:27], v3 offset:24576\n\t"
    "95:\n\t"
    "s_memtime s[90:91]\n\t"
    "s_waitcnt lgkmcnt(0)\n\t"
    "s_sub_u32 s90, s90, s78\n\t"
    "s_cmp_lt_u32 s90, s73\n\t"
    "s_cbranch_scc1 95b\n\t"
    TP(1)
    "s_memrealtime s[82:83]\n\t"
    "buffer_load_dwordx4 v[8:11], v1, %[rws], s29 offen sc1\n\t"
    "s_and_b32 s90, s24, 1\n\t"
    "s_lshl_b32 s90, s90, 6\n\t"
    "s_bitcmp1_b32 %[outw], 11\n\t"
    "s_cselect_b32 s91, 96, 0\n\t"
    "s_lshl4_add_u32 s91, s24, s91\n\t"
    "s_load_dwordx2 s[68:69], %[ka], s90 offset:24\n\t"
    "s_load_dwordx2 s[74:75], %[ka], s91 offset:168\n\t"
    "s_sub_u32 s90, %[clus], %[mine]\n\t"
    "s_add_u32 s42, s31, s90\n\t"
    "s_add_u32 s43, s32, s90\n\t"
    "s_lshl_b32 s90, %[w], 8\n\t"
    "s_sub_u32 s90, s31, s90\n\t"
    "s_add_u32 s44, s90, 1536\n\t"
    "s_and_b32 s90, s24, 6\n\t"
    "s_cmp_eq_u32 s90, 2\n\t"
    "s_cselect_b32 s40, 3584, 0\n\t"
    "s_cmp_lt_u32 s24, 4\n\t"
    "s_cselect_b32 s90, 12288, 0\n\t"
    "s_add_u32 s80, %[ldsw], s90\n\t"
    "s_cmp_gt_u32 s24, 2\n\t"
    "s_cselect_b32 s93, 1, 0\n\t"
    "s_cselect_b32 s46, 4, 68\n\t"
    "s_cmp_eq_u32 s24, 2\n\t"
    "s_cselect_b32 s46, 20, s46\n\t"
    "s_add_u32 s81, s24, 1\n\t"
    "s_lshl2_add_u32 s90, s93, %[w]\n\t"
    "s_lshl_b32 s90, s90, 9\n\t"
    "s_add_u32 s50, s90, 159552\n\t"
    "s_lshl1_add_u32 s90, s93, s24\n\t"
    "s_mul_i32 s90, s90, 1792\n\t"
    "s_add_u32 s51, s90, %[ybase]\n\t"
    "s_add_u32 s90, s90, %[mine]\n\t"
    "s_add_u32 s76, s90, 1664\n\t"
    "s_mov_b32 s47, 157952\n\t"
    "s_mul_i32 s48, %[w], 384\n\t"
    "s_add_u32 s48, s48, s47\n\t"
    "s_lshl4_add_u32 s49, %[w], 159488\n\t"
    "s_bitcmp1_b32 s25, 2\n\t"
    "s_cbranch_scc1 10f\n\t"
    "v_and_b32 v17, 3, v0\n\t"
    "v_lshlrev_b32 v17, 4, v17\n\t"
    "v_lshrrev_b32 v6, 2, v0\n\t"
    "v_lshl_add_u32 v6, v6, 10, v17\n\t"
    "11:\n\t"
    "s_waitcnt vmcnt(0)\n\t"
    "1:\n\t"
    "v_max3_u32 v2, v8, v9, v10\n\t"
    "v_max_u32 v2, v2, v11\n\t"
    "v_cmp_eq_u32 vcc, -1, v2\n\t"
    "s_cbranch_vccnz 21f\n\t"
    "2:\n\t"
    TP(2)
    "s_bitcmp1_b32 s25, 0\n\t"
    "s_cbranch_scc1 5f\n\t"
    TP(10)
    "v_mfma_f32_16x16x32_bf16 v[56:59], a[0:3], v[8:11], 0\n\t"
    "v_mov_b32_dpp v12, v8 row_ror:12 row_mask:0xf bank_mask:0xf\n\t"
    "v_mov_b32_dpp v13, v9 row_ror:12 row_mask:0xf bank_mask:0xf\n\t"
    "v_mfma_f32_16x16x32_bf16 v[60:63], a[16:19], v[8:11], 0\n\t"
    "v_mov_b32_dpp v14, v10 row_ror:12 row_mask:0xf bank_mask:0xf\n\t"
    "v_mov_b32_dpp v15, v11 row_ror:12 row_mask:0xf bank_mask:0xf\n\t"
    "v_mov_b32_dpp v16, v8 row_ror:8 row_mask:0xf bank_mask:0xf\n\t"
    "v_mov_b32_dpp v17, v9 row_ror:8 row_mask:0xf bank_mask:0xf\n\t"
    "v_mfma_f32_16x16x32_bf16 v[56:59], a[4:7], v[12:15], v[56:59]\n\t"
    "v_mov_b32_dpp v18, v10 row_ror:8 row_mask:0xf bank_mask:0xf\n\t"
    "v_mov_b32_dpp v19, v11 row_ror:8 row_mask:0xf bank_mask:0xf\n\t"
    "v_mfma_f32_16x16x32_bf16 v[60:63], a[20:23], v[12:15], v[60:63]\n\t"
    "v_mov_b32_dpp v20, v8 row_ror:4 row_mask:0xf bank_mask:0xf\n\t"
    "v_mov_b32_dpp v21, v9 row_ror:4 row_mask:0xf bank_mask:0xf\n\t"
    "v_mfma_f32_16x16x32_bf16 v[56:59], a[8:11], v[16:19], v[56:59]\n\t"
    "v_mov_b32_dpp v22, v10 row_ror:4 row_mask:0xf bank_mask:0xf\n\t"
    "v_mov_b32_dpp v23, v11 row_ror:4 row_mask:0xf bank_mask:0xf\n\t"
    "v_mfma_f32_16x16x32_bf16 v[60:63], a[24:27], v[16:19], v[60:63]\n\t"
    "s_bitcmp1_b32 s25, 2\n\t"
    "v_mfma_f32_16x16x32_bf16 v[56:59], a[12:15], v[20:23], v[56:59]\n\t"
    "s_cselect_b64 vcc, -1, 0\n\t"
    "v_mfma_f32_16x16x32_bf16 v[60:63], a[28:31], v[20:23], v[60:63]\n\t"
    "s_cbranch_scc1 40f\n\t"
    "v_mfma_f32_16x16x32_bf16 v[32:35], a[36:39], v[8:11], 0\n\t"
    "s_add_u32 m0, %[ldsw], 12288\n\t"
    "s_add_u32 s90, %[wo_off], 0\n\t"
    "v_mfma_f32_16x16x32_bf16 v[32:35], a[40:43], v[12:15], v[32:35]\n\t"
    "buffer_load_dwordx4 v6, s[60:63], s90 offen lds\n\t"
    "v_mfma_f32_16x16x32_bf16 v[32:35], a[44:47], v[16:19], v[32:35]\n\t"
    "v_mov_b32 v0, 0x3f803f80\n\t"
    "v_mov_b32 v1, 0x3f803f80\n\t"
    "v_mfma_f32_16x16x32_bf16 v[32:35], a[32:35], v[20:23], v[32:35]\n\t"
    "s_add_u32 m0, %[ldsw], 13312\n\t"
    "s_add_u32 s90, %[wo_off], 16384\n\t"
    "v_mfma_f32_16x16x32_bf16 v[36:39], a[52:55], v[8:11], 0\n\t"
    "buffer_load_dwordx4 v6, s[60:63], s90 offen lds\n\t"
    "v_mfma_f32_16x16x32_bf16 v[36:39], a[56:59], v[12:15], v[36:39]\n\t"
    "v_mov_b32 v2, 0x3f803f80\n\t"
    "v_mov_b32 v3, 0x3f803f80\n\t"
    "v_mfma_f32_16x16x32_bf16 v[36:39], a[60:63], v[16:19], v[36:39]\n\t"
    "s_add_u32 m0, %[ldsw], 14336\n\t"
    "v_mfma_f32_16x16x32_bf16 v[36:39], a[48:51], v[20:23], v[36:39]\n\t"
    "s_add_u32 s90, %[wo_off], 32768\n\t"
    "v_mfma_f32_16x16x32_bf16 v[40:43], a[72:75], v[8:11], 0\n\t"
    "v_cndmask_b32_e64 v56, v56, v32, s[84:85]\n\t"
    "buffer_load_dwordx4 v6, s[60:63], s90 offen lds\n\t"
    "v_mfma_f32_16x16x32_bf16 v[40:43], a[76:79], v[12:15], v[40:43]\n\t"
    "v_cndmask_b32_e64 v57, v57, v33, s[84:85]\n\t"
    "s_add_u32 m0, %[ldsw], 15360\n\t"
    "v_mfma_f32_16x16x32_bf16 v[40:43], a[64:67], v[16:19], v[40:43]\n\t"
    "v_cndmask_b32_e64 v58, v58, v34, s[84:85]\n\t"
    "s_add_u32 s90, %[wo_off], 49152\n\t"
    "v_mfma_f32_16x16x32_bf16 v[40:43], a[68:71], v[20:23], v[40:43]\n\t"
    "v_cndmask_b32_e64 v59, v59, v35, s[84:85]\n\t"
    "buffer_load_dwordx4 v6, s[60:63], s90 offen lds\n\t"
    "v_mfma_f32_16x16x32_bf16 v[32:35], a[88:91], v[8:11], 0\n\t"
    "v_cndmask_b32_e64 v60, v60, v36, s[84:85]\n\t"
    "s_add_u32 m0, %[ldsw], 16384\n\t"
    "v_mfma_f32_16x16x32_bf16 v[32:35], a[92:95], v[12:15], v[32:35]\n\t"
    "v_cndmask_b32_e64 v61, v61, v37, s[84:85]\n\t"
    "s_add_u32 s90, %[wo_off], 65536\n\t"
    "v_mfma_f32_16x16x32_bf16 v[32:35], a[80:83], v[16:19], v[32:35]\n\t"
    "v_cndmask_b32_e64 v62, v62, v38, s[84:85]\n\t"
    "buffer_load_dwordx4 v6, s[60:63], s90 offen lds\n\t"
    "v_mfma_f32_16x16x32_bf16 v[32:35], a[84:87], v[20:23], v[32:35]\n\t"
    "v_cndmask_b32_e64 v63, v63, v39, s[84:85]\n\t"
    "s_add_u32 m0, %[ldsw], 17408\n\t"
    "v_mfma_f32_16x16x32_bf16 v[36:39], a[108:111], v[8:11], 0\n\t"
    "v_cndmask_b32_e64 v56, v56, v40, s[86:87]\n\t"
    "s_add_u32 s90, %[wo_off], 81920\n\t"
    "v_mfma_f32_16x16x32_bf16 v[36:39], a[96:99], v[12:15], v[36:39]\n\t"
    "v_cndmask_b32_e64 v57, v57, v41, s[86:87]\n\t"
    "buffer_load_dwordx4 v6, s[60:63], s90 offen lds\n\t"
    "v_mfma_f32_16x16x32_bf16 v[36:39], a[100:103], v[16:19], v[36:39]\n\t"
    "v_cndmask_b32_e64 v58, v58, v42, s[86:87]\n\t"
    "s_add_u32 m0, %[ldsw], 18432\n\t"
    "v_mfma_f32_16x16x32_bf16 v[36:39], a[104:107], v[20:23], v[36:39]\n\t"
    "v_cndmask_b32_e64 v59, v59, v43, s[86:87]\n\t"
    "s_add_u32 s90, %[wo_off], 98304\n\t"
    "v_mfma_f32_16x16x32_bf16 v[40:43], a[124:127], v[8:11], 0\n\t"
    "v_cndmask_b32_e64 v60, v60, v32, s[86:87]\n\t"
    "buffer_load_dwordx4 v6, s[60:63], s90 offen lds\n\t"
    "v_mfma_f32_16x16x32_bf16 v[40:43], a[112:115], v[12:15], v[40:43]\n\t"
    "v_cndmask_b32_e64 v61, v61, v33, s[86:87]\n\t"
    "s_add_u32 m0, %[ldsw], 19456\n\t"
    "v_mfma_f32_16x16x32_bf16 v[40:43], a[116:119], v[16:19], v[40:43]\n\t"
    "v_cndmask_b32_e64 v62, v62, v34, s[86:87]\n\t"
    "s_add_u32 s90, %[wo_off], 114688\n\t"
    "v_mfma_f32_16x16x32_bf16 v[40:43], a[120:123], v[20:23], v[40:43]\n\t"
    "v_cndmask_b32_e64 v63, v63, v35, s[86:87]\n\t"
    "buffer_load_dwordx4 v6, s[60:63], s90 offen lds\n\t"
    "41:\n\t"
    TP(11)
    "v_cndmask_b32_e64 v56, v56, v36, s[88:89]\n\t"
    "v_cndmask_b32_e64 v57, v57, v37, s[88:89]\n\t"
    "v_cndmask_b32_e64 v58, v58, v38, s[88:89]\n\t"
    "v_cndmask_b32_e64 v59, v59, v39, s[88:89]\n\t"
    "v_max3_f32 v6, v56, v57, v58\n\t"
    "v_max_f32 v6, v6, v59\n\t"
    "ds_read_b64_tr_b16 v[32:33], v31 offset:20480\n\t"
    "ds_read_b64_tr_b16 v[34:35], v31 offset:24576\n\t"
    "v_cndmask_b32_e64 v60, v60, v40, s[88:89]\n\t"
    "v_cndmask_b32_e64 v61, v61, v41, s[88:89]\n\t"
    "v_cndmask_b32_e64 v62, v62, v42, s[88:89]\n\t"
    "v_cndmask_b32_e64 v63, v63, v43, s[88:89]\n\t"
    "v_max3_f32 v6, v6, v60, v61\n\t"
    "v_max3_f32 v6, v6, v62, v63\n\t"
    "v_xor_b32 v12, 192, v31\n\t"
    "ds_read_b64_tr_b16 v[20:21], v12 offset:20480\n\t"
    "v_max_f32_dpp v6, v6, v6 row_ror:4 row_mask:0xf bank_mask:0xf\n\t"
    "ds_read_b64_tr_b16 v[22:23], v12 offset:24576\n\t"
    "v_xor_b32 v13, 32, v31\n\t"
    "v_max_f32_dpp v6, v6, v6 row_ror:8 row_mask:0xf bank_mask:0xf\n\t"
    "v_mov_b32 v7, v6\n\t"
    "ds_read_b64_tr_b16 v[36:37], v13 offset:20480\n\t"
    "ds_read_b64_tr_b16 v[38:39], v13 offset:24576\n\t"
    "v_permlane16_swap_b32 v6, v7\n\t"
    "v_max_f32 v6, v6, v7\n\t"
    "v_mov_b32 v7, v6\n\t"
    "v_xor_b32 v12, 64, v31\n\t"
    "ds_read_b64_tr_b16 v[40:41], v12 offset:20480\n\t"
    "v_permlane32_swap_b32 v6, v7\n\t"
    "v_max_f32 v6, v6, v7\n\t"
    "v_mul_f32 v6, s28, v6\n\t"
    "ds_read_b64_tr_b16 v[42:43], v12 offset:24576\n\t"
    "v_fma_f32 v56, v56, s28, -v6\n\t"
    "v_fma_f32 v57, v57, s28, -v6\n\t"
    "v_fma_f32 v58, v58, s28, -v6\n\t"
    "v_fma_f32 v59, v59, s28, -v6\n\t"
    "v_fma_f32 v60, v60, s28, -v6\n\t"
    "v_fma_f32 v61, v61, s28, -v6\n\t"
    "v_fma_f32 v62, v62, s28, -v6\n\t"
    "v_fma_f32 v63, v63, s28, -v6\n\t"
    "v_exp_f32 v56, v56\n\t"
    "v_exp_f32 v57, v57\n\t"
    "v_exp_f32 v58, v58\n\t"
    "v_exp_f32 v59, v59\n\t"
    "v_exp_f32 v60, v60\n\t"
    "v_exp_f32 v61, v61\n\t"
    "v_exp_f32 v62, v62\n\t"
    "v_exp_f32 v63, v63\n\t"
    "v_cvt_pk_bf16_f32 v8, v56, v57\n\t"
    "v_cvt_pk_bf16_f32 v9, v58, v59\n\t"
    "v_cvt_pk_bf16_f32 v10, v60, v61\n\t"
    "v_cvt_pk_bf16_f32 v11, v62, v63\n\t"
    TP(12)
    "v_and_b32 v62, 12, v29\n\t"
    "s_waitcnt lgkmcnt(0)\n\t"
    "v_mfma_f32_16x16x32_bf16 v[56:59], v[0:3], v[8:11], 0\n\t"
    "v_mov_b32_dpp v12, v8 row_ror:12 row_mask:0xf bank_mask:0xf\n\t"
    "v_mov_b32_dpp v13, v9 row_ror:12 row_mask:0xf bank_mask:0xf\n\t"
    "v_mfma_f32_16x16x32_bf16 v[0:3], v[32:35], v[8:11], 0\n\t"
    "v_mov_b32_dpp v14, v10 row_ror:12 row_mask:0xf bank_mask:0xf\n\t"
    "v_mov_b32_dpp v15, v11 row_ror:12 row_mask:0xf bank_mask:0xf\n\t"
    "v_mfma_f32_16x16x32_bf16 v[32:35], v[36:39], v[8:11], 0\n\t"
    "v_mov_b32_dpp v16, v8 row_ror:8 row_mask:0xf bank_mask:0xf\n\t"
    "v_mov_b32_dpp v17, v9 row_ror:8 row_mask:0xf bank_mask:0xf\n\t"
    "v_mfma_f32_16x16x32_bf16 v[36:39], v[40:43], v[8:11], 0\n\t"
    "v_mov_b32_dpp v18, v10 row_ror:8 row_mask:0xf bank_mask:0xf\n\t"
    "v_add_f32_dpp v7, v56, v56 row_ror:4 row_mask:0xf bank_mask:0xf\n\t"
    "v_mfma_f32_16x16x32_bf16 v[40:43], v[44:47], v[8:11], 0\n\t"
    "v_mov_b32_dpp v19, v11 row_ror:8 row_mask:0xf bank_mask:0xf\n\t"
    "v_add_f32_dpp v7, v7, v7 row_ror:8 row_mask:0xf bank_mask:0xf\n\t"
    "v_mfma_f32_16x16x32_bf16 v[44:47], v[48:51], v[8:11], 0\n\t"
    "ds_write_b64 v5, v[6:7] offset:512\n\t"
    "v_mul_u32_u24 v62, 528, v62\n\t"
    "v_mfma_f32_16x16x32_bf16 v[48:51], v[52:55], v[8:11], 0\n\t"
    "v_add_u32 v62, s27, v62\n\t"
    "v_mfma_f32_16x16x32_bf16 v[52:55], v[20:23], v[8:11], 0\n\t"
    "v_mfma_f32_16x16x32_bf16 v[20:23], v[24:27], v[8:11], 0\n\t"
    "v_mfma_f32_16x16x32_bf16 v[0:3], v[64:67], v[12:15], v[0:3]\n\t"
    "v_mov_b32_dpp v24, v8 row_ror:4 row_mask:0xf bank_mask:0xf\n\t"
    "v_mov_b32_dpp v25, v9 row_ror:4 row_mask:0xf bank_mask:0xf\n\t"
    "v_mfma_f32_16x16x32_bf16 v[32:35], v[68:71], v[12:15], v[32:35]\n\t"
    "v_mov_b32_dpp v26, v10 row_ror:4 row_mask:0xf bank_mask:0xf\n\t"
    "v_mov_b32_dpp v27, v11 row_ror:4 row_mask:0xf bank_mask:0xf\n\t"
    "s_waitcnt lgkmcnt(0)\n\t"
    "s_barrier\n\t"
    "ds_read_b64 v[60:61], v62 offset:512\n\t"
    "v_mfma_f32_16x16x32_bf16 v[36:39], v[72:75], v[12:15], v[36:39]\n\t"
    "v_mfma_f32_16x16x32_bf16 v[40:43], v[76:79], v[12:15], v[40:43]\n\t"
    "v_mfma_f32_16x16x32_bf16 v[44:47], v[80:83], v[12:15], v[44:47]\n\t"
    "v_mfma_f32_16x16x32_bf16 v[48:51], v[84:87], v[12:15], v[48:51]\n\t"
    "v_mfma_f32_16x16x32_bf16 v[52:55], v[88:91], v[12:15], v[52:55]\n\t"
    "s_waitcnt lgkmcnt(0)\n\t"
    "v_max_f32_dpp v62, v60, v60 quad_perm:[1,0,3,2] row_mask:0xf bank_mask:0xf\n\t"
    "v_mfma_f32_16x16x32_bf16 v[20:23], v[92:95], v[12:15], v[20:23]\n\t"
    "v_mfma_f32_16x16x32_bf16 v[0:3], v[96:99], v[16:19], v[0:3]\n\t"
    "v_max_f32_dpp v62, v62, v62 quad_perm:[2,3,0,1] row_mask:0xf bank_mask:0xf\n\t"
    "v_sub_f32 v60, v60, v62\n\t"
    "v_exp_f32 v60, v60\n\t"
    "v_mfma_f32_16x16x32_bf16 v[0:3], v[128:131], v[24:27], v[0:3]\n\t"
    "v_mfma_f32_16x16x32_bf16 v[32:35], v[100:103], v[16:19], v[32:35]\n\t"
    "v_mul_f32 v61, v60, v61\n\t"
    "v_mfma_f32_16x16x32_bf16 v[32:35], v[132:135], v[24:27], v[32:35]\n\t"
    "v_mfma_f32_16x16x32_bf16 v[36:39], v[104:107], v[16:19], v[36:39]\n\t"
    "v_add_f32_dpp v63, v61, v61 quad_perm:[1,0,3,2] row_mask:0xf bank_mask:0xf\n\t"
    "v_mfma_f32_16x16x32_bf16 v[36:39], v[136:139], v[24:27], v[36:39]\n\t"
    "v_mfma_f32_16x16x32_bf16 v[40:43], v[108:111], v[16:19], v[40:43]\n\t"
    "v_add_f32_dpp v63, v63, v63 quad_perm:[2,3,0,1] row_mask:0xf bank_mask:0xf\n\t"
    "v_log_f32 v61, v63\n\t"
    "v_rcp_f32 v63, v63\n\t"
    "v_mfma_f32_16x16x32_bf16 v[40:43], v[140:143], v[24:27], v[40:43]\n\t"
    "v_mov_b32_dpp v0, v32 row_shr:4 row_mask:0xf bank_mask:0x2\n\t"
    "v_mov_b32_dpp v1, v33 row_shr:4 row_mask:0xf bank_mask:0x2\n\t"
    "v_mfma_f32_16x16x32_bf16 v[44:47], v[112:115], v[16:19], v[44:47]\n\t"
    "v_mov_b32_dpp v2, v34 row_shr:4 row_mask:0xf bank_mask:0x2\n\t"
    "v_mov_b32_dpp v3, v35 row_shr:4 row_mask:0xf bank_mask:0x2\n\t"
    "v_mfma_f32_16x16x32_bf16 v[44:47], v[144:147], v[24:27], v[44:47]\n\t"
    "v_add_f32 v61, v61, v62\n\t"
    "v_mul_f32 v60, v60, v63\n\t"
    "v_min_u32 v61, -2, v61\n\t"
    "v_mfma_f32_16x16x32_bf16 v[48:51], v[116:119], v[16:19], v[48:51]\n\t"
    "s_cbranch_vccnz 42f\n\t"
    "s_waitcnt vmcnt(0)\n\t"
    "buffer_store_dword v61, v30, %[rws], s32 offen sc1\n\t"
    "43:\n\t"
    "v_mov_b32_dpp v0, v36 row_shr:8 row_mask:0xf bank_mask:0x4\n\t"
    "v_mfma_f32_16x16x32_bf16 v[48:51], v[148:151], v[24:27], v[48:51]\n\t"
    "v_mov_b32_dpp v1, v37 row_shr:8 row_mask:0xf bank_mask:0x4\n\t"
    "v_mov_b32_dpp v2, v38 row_shr:8 row_mask:0xf bank_mask:0x4\n\t"
    "v_mov_b32_dpp v3, v39 row_shr:8 row_mask:0xf bank_mask:0x4\n\t"
    "v_mfma_f32_16x16x32_bf16 v[52:55], v[120:123], v[16:19], v[52:55]\n\t"
    "v_mov_b32_dpp v0, v40 row_shr:12 row_mask:0xf bank_mask:0x8\n\t"
    "v_mov_b32_dpp v1, v41 row_shr:12 row_mask:0xf bank_mask:0x8\n\t"
    "v_mfma_f32_16x16x32_bf16 v[52:55], v[152:155], v[24:27], v[52:55]\n\t"
    "v_mov_b32_dpp v2, v42 row_shr:12 row_mask:0xf bank_mask:0x8\n\t"
    "v_mov_b32_dpp v3, v43 row_shr:12 row_mask:0xf bank_mask:0x8\n\t"
    "v_mfma_f32_16x16x32_bf16 v[20:23], v[124:127], v[16:19], v[20:23]\n\t"
    "ds_write_b128 v4, v[0:3]\n\t"
    "v_mov_b32_dpp v44, v48 row_shr:4 row_mask:0xf bank_mask:0x2\n\t"
    "v_mov_b32_dpp v45, v49 row_shr:4 row_mask:0xf bank_mask:0x2\n\t"
    "v_mfma_f32_16x16x32_bf16 v[20:23], v[156:159], v[24:27], v[20:23]\n\t"
    TP(13)
    "v_mov_b32_dpp v46, v50 row_shr:4 row_mask:0xf bank_mask:0x2\n\t"
    "v_mov_b32_dpp v47, v51 row_shr:4 row_mask:0xf bank_mask:0x2\n\t"
    "v_mov_b32_dpp v44, v52 row_shr:8 row_mask:0xf bank_mask:0x4\n\t"
    "v_mov_b32_dpp v45, v53 row_shr:8 row_mask:0xf bank_mask:0x4\n\t"
    "v_mov_b32_dpp v46, v54 row_shr:8 row_mask:0xf bank_mask:0x4\n\t"
    "v_mov_b32_dpp v47, v55 row_shr:8 row_mask:0xf bank_mask:0x4\n\t"
    "s_nop 1\n\t"
    "v_mov_b32_dpp v44, v20 row_shr:12 row_mask:0xf bank_mask:0x8\n\t"
    "v_mov_b32_dpp v45, v21 row_shr:12 row_mask:0xf bank_mask:0x8\n\t"
    "v_mov_b32_dpp v46, v22 row_shr:12 row_mask:0xf bank_mask:0x8\n\t"
    "v_mov_b32_dpp v47, v23 row_shr:12 row_mask:0xf bank_mask:0x8\n\t"
    "ds_write_b128 v4, v[44:47] offset:256\n\t"
    "s_waitcnt lgkmcnt(0)\n\t"
    TP(14)
    "s_barrier\n\t"
    TP(15)
    "s_memtime s[78:79]\n\t"
    "ds_read_b64 v[8:9], v28 offset:0\n\t"
    "ds_read_b64 v[10:11], v28 offset:2112\n\t"
    "ds_read_b64 v[12:13], v28 offset:4224\n\t"
    "ds_read_b64 v[14:15], v28 offset:6336\n\t"
    "s_waitcnt lgkmcnt(3)\n\t"
    "v_mul_f32_dpp v16, v60, v8 quad_perm:[0,0,0,0] row_mask:0xf bank_mask:0xf\n\t"
    "v_mul_f32_dpp v17, v60, v9 quad_perm:[0,0,0,0] row_mask:0xf bank_mask:0xf\n\t"
    "s_waitcnt lgkmcnt(2)\n\t"
    "v_fmac_f32_dpp v16, v60, v10 quad_perm:[1,1,1,1] row_mask:0xf bank_mask:0xf\n\t"
    "v_fmac_f32_dpp v17, v60, v11 quad_perm:[1,1,1,1] row_mask:0xf bank_mask:0xf\n\t"
    "s_waitcnt lgkmcnt(1)\n\t"
    "v_fmac_f32_dpp v16, v60, v12 quad_perm:[2,2,2,2] row_mask:0xf bank_mask:0xf\n\t"
    "v_fmac_f32_dpp v17, v60, v13 quad_perm:[2,2,2,2] row_mask:0xf bank_mask:0xf\n\t"
    "s_waitcnt lgkmcnt(0)\n\t"
    "v_fmac_f32_dpp v16, v60, v14 quad_perm:[3,3,3,3] row_mask:0xf bank_mask:0xf\n\t"
    "v_fmac_f32_dpp v17, v60, v15 quad_perm:[3,3,3,3] row_mask:0xf bank_mask:0xf\n\t"
    "v_cvt_pk_bf16_f32 v60, v16, v17\n\t"
    "v_min_u32 v60, -2, v60\n\t"
    "s_cbranch_vccnz 45f\n\t"
    "buffer_store_dword v60, v29, %[rws], s31 offen sc1\n\t"
    TP(16)
    "s_branch 99f\n\t"
    "40:\n\t"
    "v_mfma_f32_16x16x32_bf16 v[32:35], a[36:39], v[8:11], 0\n\t"
    "ds_read_b128 a[0:3], v6 offset:0\n\t"
    "v_xor_b32 v2, 16, v6\n\t"
    "ds_read_b128 a[4:7], v2 offset:0\n\t"
    "v_xor_b32 v3, 32, v6\n\t"
    "v_mfma_f32_16x16x32_bf16 v[32:35], a[40:43], v[12:15], v[32:35]\n\t"
    "ds_read_b128 a[8:11], v3 offset:0\n\t"
    "v_xor_b32 v2, 48, v6\n\t"
    "ds_read_b128 a[12:15], v2 offset:0\n\t"
    "ds_read_b128 a[16:19], v6 offset:4096\n\t"
    "v_mfma_f32_16x16x32_bf16 v[32:35], a[44:47], v[16:19], v[32:35]\n\t"
    "v_xor_b32 v2, 16, v6\n\t"
    "ds_read_b128 a[20:23], v2 offset:4096\n\t"
    "v_xor_b32 v3, 32, v6\n\t"
    "v_mfma_f32_16x16x32_bf16 v[32:35], a[32:35], v[20:23], v[32:35]\n\t"
    "ds_read_b128 a[24:27], v3 offset:4096\n\t"
    "v_xor_b32 v2, 48, v6\n\t"
    "ds_read_b128 a[28:31], v2 offset:4096\n\t"
    "v_mfma_f32_16x16x32_bf16 v[36:39], a[52:55], v[8:11], 0\n\t"
    "v_mov_b32 v0, 0x3f803f80\n\t"
    "v_mov_b32 v1, 0x3f803f80\n\t"
    "v_mfma_f32_16x16x32_bf16 v[36:39], a[56:59], v[12:15], v[36:39]\n\t"
    "v_mov_b32 v2, 0x3f803f80\n\t"
    "v_mov_b32 v3, 0x3f803f80\n\t"
    "v_mfma_f32_16x16x32_bf16 v[36:39], a[60:63], v[16:19], v[36:39]\n\t"
    "v_mfma_f32_16x16x32_bf16 v[36:39], a[48:51], v[20:23], v[36:39]\n\t"
    "s_waitcnt lgkmcnt(4)\n\t"
    "s_add_u32 m0, %[ldsw], 0\n\t"
    "s_add_u32 s90, s35, 49152\n\t"
    "v_mfma_f32_16x16x32_bf16 v[40:43], a[72:75], v[8:11], 0\n\t"
    "v_cndmask_b32_e64 v56, v56, v32, s[84:85]\n\t"
    "buffer_load_dwordx4 v7, s[52:55], s90 offen nt lds\n\t"
    "v_mfma_f32_16x16x32_bf16 v[40:43], a[76:79], v[12:15], v[40:43]\n\t"
    "v_cndmask_b32_e64 v57, v57, v33, s[84:85]\n\t"
    "v_xor_b32 v6, 16, v7\n\t"
    "v_mfma_f32_16x16x32_bf16 v[40:43], a[64:67], v[16:19], v[40:43]\n\t"
    "v_cndmask_b32_e64 v58, v58, v34, s[84:85]\n\t"
    "buffer_load_dwordx4 v6, s[52:55], s90 offen offset:1024 nt lds\n\t"
    "v_mfma_f32_16x16x32_bf16 v[40:43], a[68:71], v[20:23], v[40:43]\n\t"
    "v_cndmask_b32_e64 v59, v59, v35, s[84:85]\n\t"
    "v_xor_b32 v6, 32, v7\n\t"
    "v_mfma_f32_16x16x32_bf16 v[32:35], a[88:91], v[8:11], 0\n\t"
    "v_cndmask_b32_e64 v60, v60, v36, s[84:85]\n\t"
    "buffer_load_dwordx4 v6, s[52:55], s90 offen offset:2048 nt lds\n\t"
    "v_mfma_f32_16x16x32_bf16 v[32:35], a[92:95], v[12:15], v[32:35]\n\t"
    "v_cndmask_b32_e64 v61, v61, v37, s[84:85]\n\t"
    "v_xor_b32 v6, 48, v7\n\t"
    "v_mfma_f32_16x16x32_bf16 v[32:35], a[80:83], v[16:19], v[32:35]\n\t"
    "v_cndmask_b32_e64 v62, v62, v38, s[84:85]\n\t"
    "buffer_load_dwordx4 v6, s[52:55], s90 offen offset:3072 nt lds\n\t"
    "v_mfma_f32_16x16x32_bf16 v[32:35], a[84:87], v[20:23], v[32:35]\n\t"
    "v_cndmask_b32_e64 v63, v63, v39, s[84:85]\n\t"
    "s_waitcnt lgkmcnt(0)\n\t"
    "s_add_u32 m0, %[ldsw], 4096\n\t"
    "v_mfma_f32_16x16x32_bf16 v[36:39], a[108:111], v[8:11], 0\n\t"
    "v_cndmask_b32_e64 v56, v56, v40, s[86:87]\n\t"
    "s_add_u32 s90, s35, 65536\n\t"
    "v_mfma_f32_16x16x32_bf16 v[36:39], a[96:99], v[12:15], v[36:39]\n\t"
    "v_cndmask_b32_e64 v57, v57, v41, s[86:87]\n\t"
    "buffer_load_dwordx4 v7, s[52:55], s90 offen nt lds\n\t"
    "v_mfma_f32_16x16x32_bf16 v[36:39], a[100:103], v[16:19], v[36:39]\n\t"
    "v_cndmask_b32_e64 v58, v58, v42, s[86:87]\n\t"
    "v_xor_b32 v6, 16, v7\n\t"
    "v_mfma_f32_16x16x32_bf16 v[36:39], a[104:107], v[20:23], v[36:39]\n\t"
    "v_cndmask_b32_e64 v59, v59, v43, s[86:87]\n\t"
    "buffer_load_dwordx4 v6, s[52:55], s90 offen offset:1024 nt lds\n\t"
    "v_mfma_f32_16x16x32_bf16 v[40:43], a[124:127], v[8:11], 0\n\t"
    "v_cndmask_b32_e64 v60, v60, v32, s[86:87]\n\t"
    "v_xor_b32 v6, 32, v7\n\t"
    "v_mfma_f32_16x16x32_bf16 v[40:43], a[112:115], v[12:15], v[40:43]\n\t"
    "v_cndmask_b32_e64 v61, v61, v33, s[86:87]\n\t"
    "buffer_load_dwordx4 v6, s[52:55], s90 offen offset:2048 nt lds\n\t"
    "v_mfma_f32_16x16x32_bf16 v[40:43], a[116:119], v[16:19], v[40:43]\n\t"
    "v_cndmask_b32_e64 v62, v62, v34, s[86:87]\n\t"
    "v_xor_b32 v6, 48, v7\n\t"
    "v_mfma_f32_16x16x32_bf16 v[40:43], a[120:123], v[20:23], v[40:43]\n\t"
    "v_cndmask_b32_e64 v63, v63, v35, s[86:87]\n\t"
    "buffer_load_dwordx4 v6, s[52:55], s90 offen offset:3072 nt lds\n\t"
    "s_branch 41b\n\t"
    "42:\n\t"
    "v_mbcnt_lo_u32_b32 v56, -1, 0\n\t"
    "v_mbcnt_hi_u32_b32 v56, -1, v56\n\t"
    "v_lshrrev_b32 v58, 4, v56\n\t"
    "v_xor_b32 v58, v58, v56\n\t"
    "v_and_b32 v58, 3, v58\n\t"
    "v_lshlrev_b32 v57, 6, v58\n\t"
    "v_bfe_u32 v58, v56, 2, 2\n\t"
    "v_lshl_add_u32 v57, v58, 4, v57\n\t"
    "v_and_b32 v58, 15, v56\n\t"
    "v_lshl_add_u32 v57, v58, 8, v57\n\t"
    "v_add_u32 v57, %[ldsw], v57\n\t"
    "s_waitcnt vmcnt(8)\n\t"
    "ds_read_b128 a[32:35], v57 offset:8192\n\t"
    "v_xor_b32 v58, 16, v57\n\t"
    "ds_read_b128 a[36:39], v58 offset:8192\n\t"
    "v_xor_b32 v59, 32, v57\n\t"
    "ds_read_b128 a[40:43], v59 offset:8192\n\t"
    "v_xor_b32 v58, 48, v57\n\t"
    "ds_read_b128 a[44:47], v58 offset:8192\n\t"
    "v_lshrrev_b32 v58, 4, v56\n\t"
    "v_lshlrev_b32 v57, 2, v58\n\t"
    "v_xor_b32 v57, v57, v56\n\t"
    "v_and_b32 v57, 15, v57\n\t"
    "v_lshlrev_b32 v57, 4, v57\n\t"
    "v_lshl_add_u32 v57, v58, 8, v57\n\t"
    "s_waitcnt lgkmcnt(0)\n\t"
    "s_add_u32 m0, %[ldsw], 8192\n\t"
    "s_add_u32 s90, s35, 81920\n\t"
    "buffer_load_dwordx4 v57, s[52:55], s90 offen nt lds\n\t"
    "v_xor_b32 v58, 16, v57\n\t"
    "buffer_load_dwordx4 v58, s[52:55], s90 offen offset:1024 nt lds\n\t"
    "v_xor_b32 v58, 32, v57\n\t"
    "buffer_load_dwordx4 v58, s[52:55], s90 offen offset:2048 nt lds\n\t"
    "v_xor_b32 v58, 48, v57\n\t"
    "buffer_load_dwordx4 v58, s[52:55], s90 offen offset:3072 nt lds\n\t"
    "s_branch 43b\n\t"
    "45:\n\t"
    "ds_write_b64 v28, v[60:61]\n\t"
    "s_branch 60f\n\t"
    "5:\n\t"
    TP(10)
    "v_mfma_f32_16x16x32_bf16 v[56:59], a[128:131], v[8:11], 0\n\t"
    "v_mov_b32_dpp v12, v8 row_ror:12 row_mask:0xf bank_mask:0xf\n\t"
    "v_mov_b32_dpp v13, v9 row_ror:12 row_mask:0xf bank_mask:0xf\n\t"
    "v_mfma_f32_16x16x32_bf16 v[60:63], a[144:147], v[8:11], 0\n\t"
    "v_mov_b32_dpp v14, v10 row_ror:12 row_mask:0xf bank_mask:0xf\n\t"
    "v_mov_b32_dpp v15, v11 row_ror:12 row_mask:0xf bank_mask:0xf\n\t"
    "v_mov_b32_dpp v16, v8 row_ror:8 row_mask:0xf bank_mask:0xf\n\t"
    "v_mov_b32_dpp v17, v9 row_ror:8 row_mask:0xf bank_mask:0xf\n\t"
    "v_mfma_f32_16x16x32_bf16 v[56:59], a[132:135], v[12:15], v[56:59]\n\t"
    "v_mov_b32_dpp v18, v10 row_ror:8 row_mask:0xf bank_mask:0xf\n\t"
    "v_mov_b32_dpp v19, v11 row_ror:8 row_mask:0xf bank_mask:0xf\n\t"
    "v_mfma_f32_16x16x32_bf16 v[60:63], a[148:151], v[12:15], v[60:63]\n\t"
    "v_mov_b32_dpp v20, v8 row_ror:4 row_mask:0xf bank_mask:0xf\n\t"
    "v_mov_b32_dpp v21, v9 row_ror:4 row_mask:0xf bank_mask:0xf\n\t"
    "v_mfma_f32_16x16x32_bf16 v[56:59], a[136:139], v[16:19], v[56:59]\n\t"
    "v_mov_b32_dpp v22, v10 row_ror:4 row_mask:0xf bank_mask:0xf\n\t"
    "v_mov_b32_dpp v23, v11 row_ror:4 row_mask:0xf bank_mask:0xf\n\t"
    "v_mfma_f32_16x16x32_bf16 v[60:63], a[152:155], v[16:19], v[60:63]\n\t"
    "s_bitcmp1_b32 s25, 2\n\t"
    "v_mfma_f32_16x16x32_bf16 v[56:59], a[140:143], v[20:23], v[56:59]\n\t"
    "s_cselect_b64 vcc, -1, 0\n\t"
    "v_mfma_f32_16x16x32_bf16 v[60:63], a[156:159], v[20:23], v[60:63]\n\t"
    "s_cbranch_scc1 50f\n\t"
    "v_mfma_f32_16x16x32_bf16 v[32:35], a[164:167], v[8:11], 0\n\t"
    "s_add_u32 m0, %[ldsw], 12288\n\t"
    "s_add_u32 s90, %[wo_off], 0\n\t"
    "v_mfma_f32_16x16x32_bf16 v[32:35], a[168:171], v[12:15], v[32:35]\n\t"
    "buffer_load_dwordx4 v6, s[60:63], s90 offen lds\n\t"
    "v_mfma_f32_16x16x32_bf16 v[32:35], a[172:175], v[16:19], v[32:35]\n\t"
    "v_mov_b32 v0, 0x3f803f80\n\t"
    "v_mov_b32 v1, 0x3f803f80\n\t"
    "v_mfma_f32_16x16x32_bf16 v[32:35], a[160:163], v[20:23], v[32:35]\n\t"
    "s_add_u32 m0, %[ldsw], 13312\n\t"
    "s_add_u32 s90, %[wo_off], 16384\n\t"
    "v_mfma_f32_16x16x32_bf16 v[36:39], a[180:183], v[8:11], 0\n\t"
    "buffer_load_dwordx4 v6, s[60:63], s90 offen lds\n\t"
    "v_mfma_f32_16x16x32_bf16 v[36:39], a[184:187], v[12:15], v[36:39]\n\t"
    "v_mov_b32 v2, 0x3f803f80\n\t"
    "v_mov_b32 v3, 0x3f803f80\n\t"
    "v_mfma_f32_16x16x32_bf16 v[36:39], a[188:191], v[16:19], v[36:39]\n\t"
    "s_add_u32 m0, %[ldsw], 14336\n\t"
    "v_mfma_f32_16x16x32_bf16 v[36:39], a[176:179], v[20:23], v[36:39]\n\t"
    "s_add_u32 s90, %[wo_off], 32768\n\t"
    "v_mfma_f32_16x16x32_bf16 v[40:43], a[200:203], v[8:11], 0\n\t"
    "v_cndmask_b32_e64 v56, v56, v32, s[84:85]\n\t"
    "buffer_load_dwordx4 v6, s[60:63], s90 offen lds\n\t"
    "v_mfma_f32_16x16x32_bf16 v[40:43], a[204:207], v[12:15], v[40:43]\n\t"
    "v_cndmask_b32_e64 v57, v57, v33, s[84:85]\n\t"
    "s_add_u32 m0, %[ldsw], 15360\n\t"
    "v_mfma_f32_16x16x32_bf16 v[40:43], a[192:195], v[16:19], v[40:43]\n\t"
    "v_cndmask_b32_e64 v58, v58, v34, s[84:85]\n\t"
    "s_add_u32 s90, %[wo_off], 49152\n\t"
    "v_mfma_f32_16x16x32_bf16 v[40:43], a[196:199], v[20:23], v[40:43]\n\t"
    "v_cndmask_b32_e64 v59, v59, v35, s[84:85]\n\t"
    "buffer_load_dwordx4 v6, s[60:63], s90 offen lds\n\t"
    "v_mfma_f32_16x16x32_bf16 v[32:35], a[216:219], v[8:11], 0\n\t"
    "v_cndmask_b32_e64 v60, v60, v36, s[84:85]\n\t"
    "s_add_u32 m0, %[ldsw], 16384\n\t"
    "v_mfma_f32_16x16x32_bf16 v[32:35], a[220:223], v[12:15], v[32:35]\n\t"
    "v_cndmask_b32_e64 v61, v61, v37, s[84:85]\n\t"
    "s_add_u32 s90, %[wo_off], 65536\n\t"
    "v_mfma_f32_16x16x32_bf16 v[32:35], a[208:211], v[16:19], v[32:35]\n\t"
    "v_cndmask_b32_e64 v62, v62, v38, s[84:85]\n\t"
    "buffer_load_dwordx4 v6, s[60:63], s90 offen lds\n\t"
    "v_mfma_f32_16x16x32_bf16 v[32:35], a[212:215], v[20:23], v[32:35]\n\t"
    "v_cndmask_b32_e64 v63, v63, v39, s[84:85]\n\t"
    "s_add_u32 m0, %[ldsw], 17408\n\t"
    "v_mfma_f32_16x16x32_bf16 v[36:39], a[236:239], v[8:11], 0\n\t"
    "v_cndmask_b32_e64 v56, v56, v40, s[86:87]\n\t"
    "s_add_u32 s90, %[wo_off], 81920\n\t"
    "v_mfma_f32_16x16x32_bf16 v[36:39], a[224:227], v[12:15], v[36:39]\n\t"
    "v_cndmask_b32_e64 v57, v57, v41, s[86:87]\n\t"
    "buffer_load_dwordx4 v6, s[60:63], s90 offen lds\n\t"
    "v_mfma_f32_16x16x32_bf16 v[36:39], a[228:231], v[16:19], v[36:39]\n\t"
    "v_cndmask_b32_e64 v58, v58, v42, s[86:87]\n\t"
    "s_add_u32 m0, %[ldsw], 18432\n\t"
    "v_mfma_f32_16x16x32_bf16 v[36:39], a[232:235], v[20:23], v[36:39]\n\t"
    "v_cndmask_b32_e64 v59, v59, v43, s[86:87]\n\t"
    "s_add_u32 s90, %[wo_off], 98304\n\t"
    "v_mfma_f32_16x16x32_bf16 v[40:43], a[252:255], v[8:11], 0\n\t"
    "v_cndmask_b32_e64 v60, v60, v32, s[86:87]\n\t"
    "buffer_load_dwordx4 v6, s[60:63], s90 offen lds\n\t"
    "v_mfma_f32_16x16x32_bf16 v[40:43], a[240:243], v[12:15], v[40:43]\n\t"
    "v_cndmask_b32_e64 v61, v61, v33, s[86:87]\n\t"
    "s_add_u32 m0, %[ldsw], 19456\n\t"
    "v_mfma_f32_16x16x32_bf16 v[40:43], a[244:247], v[16:19], v[40:43]\n\t"
    "v_cndmask_b32_e64 v62, v62, v34, s[86:87]\n\t"
    "s_add_u32 s90, %[wo_off], 114688\n\t"
    "v_mfma_f32_16x16x32_bf16 v[40:43], a[248:251], v[20:23], v[40:43]\n\t"
    "v_cndmask_b32_e64 v63, v63, v35, s[86:87]\n\t"
    "buffer_load_dwordx4 v6, s[60:63], s90 offen lds\n\t"
    "51:\n\t"
    TP(11)
    "v_cndmask_b32_e64 v56, v56, v36, s[88:89]\n\t"
    "v_cndmask_b32_e64 v57, v57, v37, s[88:89]\n\t"
    "v_cndmask_b32_e64 v58, v58, v38, s[88:89]\n\t"
    "v_cndmask_b32_e64 v59, v59, v39, s[88:89]\n\t"
    "v_max3_f32 v6, v56, v57, v58\n\t"
    "v_max_f32 v6, v6, v59\n\t"
    "ds_read_b64_tr_b16 v[32:33], v31 offset:20480\n\t"
    "ds_read_b64_tr_b16 v[34:35], v31 offset:24576\n\t"
    "v_cndmask_b32_e64 v60, v60, v40, s[88:89]\n\t"
    "v_cndmask_b32_e64 v61, v61, v41, s[88:89]\n\t"
    "v_cndmask_b32_e64 v62, v62, v42, s[88:89]\n\t"
    "v_cndmask_b32_e64 v63, v63, v43, s[88:89]\n\t"
    "v_max3_f32 v6, v6, v60, v61\n\t"
    "v_max3_f32 v6, v6, v62, v63\n\t"
    "v_xor_b32 v12, 192, v31\n\t"
    "ds_read_b64_tr_b16 v[20:21], v12 offset:20480\n\t"
    "v_max_f32_dpp v6, v6, v6 row_ror:4 row_mask:0xf bank_mask:0xf\n\t"
    "ds_read_b64_tr_b16 v[22:23], v12 offset:24576\n\t"
    "v_xor_b32 v13, 32, v31\n\t"
    "v_max_f32_dpp v6, v6, v6 row_ror:8 row_mask:0xf bank_mask:0xf\n\t"
    "v_mov_b32 v7, v6\n\t"
    "ds_read_b64_tr_b16 v[36:37], v13 offset:20480\n\t"
    "ds_read_b64_tr_b16 v[38:39], v13 offset:24576\n\t"
    "v_permlane16_swap_b32 v6, v7\n\t"
    "v_max_f32 v6, v6, v7\n\t"
    "v_mov_b32 v7, v6\n\t"
    "v_xor_b32 v12, 64, v31\n\t"
    "ds_read_b64_tr_b16 v[40:41], v12 offset:20480\n\t"
    "v_permlane32_swap_b32 v6, v7\n\t"
    "v_max_f32 v6, v6, v7\n\t"
    "v_mul_f32 v6, s28, v6\n\t"
    "ds_read_b64_tr_b16 v[42:43], v12 offset:24576\n\t"
    "v_fma_f32 v56, v56, s28, -v6\n\t"
    "v_fma_f32 v57, v57, s28, -v6\n\t"
    "v_fma_f32 v58, v58, s28, -v6\n\t"
    "v_fma_f32 v59, v59, s28, -v6\n\t"
    "v_fma_f32 v60, v60, s28, -v6\n\t"
    "v_fma_f32 v61, v61, s28, -v6\n\t"
    "v_fma_f32 v62, v62, s28, -v6\n\t"
    "v_fma_f32 v63, v63, s28, -v6\n\t"
    "v_exp_f32 v56, v56\n\t"
    "v_exp_f32 v57, v57\n\t"
    "v_exp_f32 v58, v58\n\t"
    "v_exp_f32 v59, v59\n\t"
    "v_exp_f32 v60, v60\n\t"
    "v_exp_f32 v61, v61\n\t"
    "v_exp_f32 v62, v62\n\t"
    "v_exp_f32 v63, v63\n\t"
    "v_cvt_pk_bf16_f32 v8, v56, v57\n\t"
    "v_cvt_pk_bf16_f32 v9, v58, v59\n\t"
    "v_cvt_pk_bf16_f32 v10, v60, v61\n\t"
    "v_cvt_pk_bf16_f32 v11, v62, v63\n\t"
    TP(12)
    "v_and_b32 v62, 12, v29\n\t"
    "s_waitcnt lgkmcnt(0)\n\t"
    "v_mfma_f32_16x16x32_bf16 v[56:59], v[0:3], v[8:11], 0\n\t"
    "v_mov_b32_dpp v12, v8 row_ror:12 row_mask:0xf bank_mask:0xf\n\t"
    "v_mov_b32_dpp v13, v9 row_ror:12 row_mask:0xf bank_mask:0xf\n\t"
    "v_mfma_f32_16x16x32_bf16 v[0:3], v[32:35], v[8:11], 0\n\t"
    "v_mov_b32_dpp v14, v10 row_ror:12 row_mask:0xf bank_mask:0xf\n\t"
    "v_mov_b32_dpp v15, v11 row_ror:12 row_mask:0xf bank_mask:0xf\n\t"
    "v_mfma_f32_16x16x32_bf16 v[32:35], v[36:39], v[8:11], 0\n\t"
    "v_mov_b32_dpp v16, v8 row_ror:8 row_mask:0xf bank_mask:0xf\n\t"
    "v_mov_b32_dpp v17, v9 row_ror:8 row_mask:0xf bank_mask:0xf\n\t"
    "v_mfma_f32_16x16x32_bf16 v[36:39], v[40:43], v[8:11], 0\n\t"
    "v_mov_b32_dpp v18, v10 row_ror:8 row_mask:0xf bank_mask:0xf\n\t"
    "v_add_f32_dpp v7, v56, v56 row_ror:4 row_mask:0xf bank_mask:0xf\n\t"
    "v_mfma_f32_16x16x32_bf16 v[40:43], v[44:47], v[8:11], 0\n\t"
    "v_mov_b32_dpp v19, v11 row_ror:8 row_mask:0xf bank_mask:0xf\n\t"
    "v_add_f32_dpp v7, v7, v7 row_ror:8 row_mask:0xf bank_mask:0xf\n\t"
    "v_mfma_f32_16x16x32_bf16 v[44:47], v[48:51], v[8:11], 0\n\t"
    "ds_write_b64 v5, v[6:7] offset:512\n\t"
    "v_mul_u32_u24 v62, 528, v62\n\t"
    "v_mfma_f32_16x16x32_bf16 v[48:51], v[52:55], v[8:11], 0\n\t"
    "v_add_u32 v62, s27, v62\n\t"
    "v_mfma_f32_16x16x32_bf16 v[52:55], v[20:23], v[8:11], 0\n\t"
    "v_mfma_f32_16x16x32_bf16 v[20:23], v[24:27], v[8:11], 0\n\t"
    "v_mfma_f32_16x16x32_bf16 v[0:3], v[160:163], v[12:15], v[0:3]\n\t"
    "v_mov_b32_dpp v24, v8 row_ror:4 row_mask:0xf bank_mask:0xf\n\t"
    "v_mov_b32_dpp v25, v9 row_ror:4 row_mask:0xf bank_mask:0xf\n\t"
    "v_mfma_f32_16x16x32_bf16 v[32:35], v[164:167], v[12:15], v[32:35]\n\t"
    "v_mov_b32_dpp v26, v10 row_ror:4 row_mask:0xf bank_mask:0xf\n\t"
    "v_mov_b32_dpp v27, v11 row_ror:4 row_mask:0xf bank_mask:0xf\n\t"
    "s_waitcnt lgkmcnt(0)\n\t"
    "s_barrier\n\t"
    "ds_read_b64 v[60:61], v62 offset:512\n\t"
    "v_mfma_f32_16x16x32_bf16 v[36:39], v[168:171], v[12:15], v[36:39]\n\t"
    "v_mfma_f32_16x16x32_bf16 v[40:43], v[172:175], v[12:15], v[40:43]\n\t"
    "v_mfma_f32_16x16x32_bf16 v[44:47], v[176:179], v[12:15], v[44:47]\n\t"
    "v_mfma_f32_16x16x32_bf16 v[48:51], v[180:183], v[12:15], v[48:51]\n\t"
    "v_mfma_f32_16x16x32_bf16 v[52:55], v[184:187], v[12:15], v[52:55]\n\t"
    "s_waitcnt lgkmcnt(0)\n\t"
    "v_max_f32_dpp v62, v60, v60 quad_perm:[1,0,3,2] row_mask:0xf bank_mask:0xf\n\t"
    "v_mfma_f32_16x16x32_bf16 v[20:23], v[188:191], v[12:15], v[20:23]\n\t"
    "v_mfma_f32_16x16x32_bf16 v[0:3], v[192:195], v[16:19], v[0:3]\n\t"
    "v_max_f32_dpp v62, v62, v62 quad_perm:[2,3,0,1] row_mask:0xf bank_mask:0xf\n\t"
    "v_sub_f32 v60, v60, v62\n\t"
    "v_exp_f32 v60, v60\n\t"
    "v_mfma_f32_16x16x32_bf16 v[0:3], v[224:227], v[24:27], v[0:3]\n\t"
    "v_mfma_f32_16x16x32_bf16 v[32:35], v[196:199], v[16:19], v[32:35]\n\t"
    "v_mul_f32 v61, v60, v61\n\t"
    "v_mfma_f32_16x16x32_bf16 v[32:35], v[228:231], v[24:27], v[32:35]\n\t"
    "v_mfma_f32_16x16x32_bf16 v[36:39], v[200:203], v[16:19], v[36:39]\n\t"
    "v_add_f32_dpp v63, v61, v61 quad_perm:[1,0,3,2] row_mask:0xf bank_mask:0xf\n\t"
    "v_mfma_f32_16x16x32_bf16 v[36:39], v[232:235], v[24:27], v[36:39]\n\t"
    "v_mfma_f32_16x16x32_bf16 v[40:43], v[204:207], v[16:19], v[40:43]\n\t"
    "v_add_f32_dpp v63, v63, v63 quad_perm:[2,3,0,1] row_mask:0xf bank_mask:0xf\n\t"
    "v_log_f32 v61, v63\n\t"
    "v_rcp_f32 v63, v63\n\t"
    "v_mfma_f32_16x16x32_bf16 v[40:43], v[236:239], v[24:27], v[40:43]\n\t"
    "v_mov_b32_dpp v0, v32 row_shr:4 row_mask:0xf bank_mask:0x2\n\t"
    "v_mov_b32_dpp v1, v33 row_shr:4 row_mask:0xf bank_mask:0x2\n\t"
    "v_mfma_f32_16x16x32_bf16 v[44:47], v[208:211], v[16:19], v[44:47]\n\t"
    "v_mov_b32_dpp v2, v34 row_shr:4 row_mask:0xf bank_mask:0x2\n\t"
    "v_mov_b32_dpp v3, v35 row_shr:4 row_mask:0xf bank_mask:0x2\n\t"
    "v_mfma_f32_16x16x32_bf16 v[44:47], v[240:243], v[24:27], v[44:47]\n\t"
    "v_add_f32 v61, v61, v62\n\t"
    "v_mul_f32 v60, v60, v63\n\t"
    "v_min_u32 v61, -2, v61\n\t"
    "v_mfma_f32_16x16x32_bf16 v[48:51], v[212:215], v[16:19], v[48:51]\n\t"
    "s_cbranch_vccnz 52f\n\t"
    "s_waitcnt vmcnt(0)\n\t"
    "buffer_store_dword v61, v30, %[rws], s32 offen sc1\n\t"
    "53:\n\t"
    "v_mov_b32_dpp v0, v36 row_shr:8 row_mask:0xf bank_mask:0x4\n\t"
    "v_mfma_f32_16x16x32_bf16 v[48:51], v[244:247], v[24:27], v[48:51]\n\t"
    "v_mov_b32_dpp v1, v37 row_shr:8 row_mask:0xf bank_mask:0x4\n\t"
    "v_mov_b32_dpp v2, v38 row_shr:8 row_mask:0xf bank_mask:0x4\n\t"
    "v_mov_b32_dpp v3, v39 row_shr:8 row_mask:0xf bank_mask:0x4\n\t"
    "v_mfma_f32_16x16x32_bf16 v[52:55], v[216:219], v[16:19], v[52:55]\n\t"
    "v_mov_b32_dpp v0, v40 row_shr:12 row_mask:0xf bank_mask:0x8\n\t"
    "v_mov_b32_dpp v1, v41 row_shr:12 row_mask:0xf bank_mask:0x8\n\t"
    "v_mfma_f32_16x16x32_bf16 v[52:55], v[248:251], v[24:27], v[52:55]\n\t"
    "v_mov_b32_dpp v2, v42 row_shr:12 row_mask:0xf bank_mask:0x8\n\t"
    "v_mov_b32_dpp v3, v43 row_shr:12 row_mask:0xf bank_mask:0x8\n\t"
    "v_mfma_f32_16x16x32_bf16 v[20:23], v[220:223], v[16:19], v[20:23]\n\t"
    "ds_write_b128 v4, v[0:3]\n\t"
    "v_mov_b32_dpp v44, v48 row_shr:4 row_mask:0xf bank_mask:0x2\n\t"
    "v_mov_b32_dpp v45, v49 row_shr:4 row_mask:0xf bank_mask:0x2\n\t"
    "v_mfma_f32_16x16x32_bf16 v[20:23], v[252:255], v[24:27], v[20:23]\n\t"
    TP(13)
    "v_mov_b32_dpp v46, v50 row_shr:4 row_mask:0xf bank_mask:0x2\n\t"
    "v_mov_b32_dpp v47, v51 row_shr:4 row_mask:0xf bank_mask:0x2\n\t"
    "v_mov_b32_dpp v44, v52 row_shr:8 row_mask:0xf bank_mask:0x4\n\t"
    "v_mov_b32_dpp v45, v53 row_shr:8 row_mask:0xf bank_mask:0x4\n\t"
    "v_mov_b32_dpp v46, v54 row_shr:8 row_mask:0xf bank_mask:0x4\n\t"
    "v_mov_b32_dpp v47, v55 row_shr:8 row_mask:0xf bank_mask:0x4\n\t"
    "s_nop 1\n\t"
    "v_mov_b32_dpp v44, v20 row_shr:12 row_mask:0xf bank_mask:0x8\n\t"
    "v_mov_b32_dpp v45, v21 row_shr:12 row_mask:0xf bank_mask:0x8\n\t"
    "v_mov_b32_dpp v46, v22 row_shr:12 row_mask:0xf bank_mask:0x8\n\t"
    "v_mov_b32_dpp v47, v23 row_shr:12 row_mask:0xf bank_mask:0x8\n\t"
    "ds_write_b128 v4, v[44:47] offset:256\n\t"
    "s_waitcnt lgkmcnt(0)\n\t"
    TP(14)
    "s_barrier\n\t"
    TP(15)
    "s_memtime s[78:79]\n\t"
    "ds_read_b64 v[8:9], v28 offset:0\n\t"
    "ds_read_b64 v[10:11], v28 offset:2112\n\t"
    "ds_read_b64 v[12:13], v28 offset:4224\n\t"
    "ds_read_b64 v[14:15], v28 offset:6336\n\t"
    "s_waitcnt lgkmcnt(3)\n\t"
    "v_mul_f32_dpp v16, v60, v8 quad_perm:[0,0,0,0] row_mask:0xf bank_mask:0xf\n\t"
    "v_mul_f32_dpp v17, v60, v9 quad_perm:[0,0,0,0] row_mask:0xf bank_mask:0xf\n\t"
    "s_waitcnt lgkmcnt(2)\n\t"
    "v_fmac_f32_dpp v16, v60, v10 quad_perm:[1,1,1,1] row_mask:0xf bank_mask:0xf\n\t"
    "v_fmac_f32_dpp v17, v60, v11 quad_perm:[1,1,1,1] row_mask:0xf bank_mask:0xf\n\t"
    "s_waitcnt lgkmcnt(1)\n\t"
    "v_fmac_f32_dpp v16, v60, v12 quad_perm:[2,2,2,2] row_mask:0xf bank_mask:0xf\n\t"
    "v_fmac_f32_dpp v17, v60, v13 quad_perm:[2,2,2,2] row_mask:0xf bank_mask:0xf\n\t"
    "s_waitcnt lgkmcnt(0)\n\t"
    "v_fmac_f32_dpp v16, v60, v14 quad_perm:[3,3,3,3] row_mask:0xf bank_mask:0xf\n\t"
    "v_fmac_f32_dpp v17, v60, v15 quad_perm:[3,3,3,3] row_mask:0xf bank_mask:0xf\n\t"
    "v_cvt_pk_bf16_f32 v60, v16, v17\n\t"
    "v_min_u32 v60, -2, v60\n\t"
    "s_cbranch_vccnz 55f\n\t"
    "buffer_store_dword v60, v29, %[rws], s31 offen sc1\n\t"
    TP(16)
    "s_branch 99f\n\t"
    "50:\n\t"
    "v_mfma_f32_16x16x32_bf16 v[32:35], a[164:167], v[8:11], 0\n\t"
    "ds_read_b128 a[128:131], v6 offset:0\n\t"
    "v_xor_b32 v2, 16, v6\n\t"
    "ds_read_b128 a[132:135], v2 offset:0\n\t"
    "v_xor_b32 v3, 32, v6\n\t"
    "v_mfma_f32_16x16x32_bf16 v[32:35], a[168:171], v[12:15], v[32:35]\n\t"
    "ds_read_b128 a[136:139], v3 offset:0\n\t"
    "v_xor_b32 v2, 48, v6\n\t"
    "ds_read_b128 a[140:143], v2 offset:0\n\t"
    "ds_read_b128 a[144:147], v6 offset:4096\n\t"
    "v_mfma_f32_16x16x32_bf16 v[32:35], a[172:175], v[16:19], v[32:35]\n\t"
    "v_xor_b32 v2, 16, v6\n\t"
    "ds_read_b128 a[148:151], v2 offset:4096\n\t"
    "v_xor_b32 v3, 32, v6\n\t"
    "v_mfma_f32_16x16x32_bf16 v[32:35], a[160:163], v[20:23], v[32:35]\n\t"
    "ds_read_b128 a[152:155], v3 offset:4096\n\t"
    "v_xor_b32 v2, 48, v6\n\t"
    "ds_read_b128 a[156:159], v2 offset:4096\n\t"
    "v_mfma_f32_16x16x32_bf16 v[36:39], a[180:183], v[8:11], 0\n\t"
    "v_mov_b32 v0, 0x3f803f80\n\t"
    "v_mov_b32 v1, 0x3f803f80\n\t"
    "v_mfma_f32_16x16x32_bf16 v[36:39], a[184:187], v[12:15], v[36:39]\n\t"
    "v_mov_b32 v2, 0x3f803f80\n\t"
    "v_mov_b32 v3, 0x3f803f80\n\t"
    "v_mfma_f32_16x16x32_bf16 v[36:39], a[188:191], v[16:19], v[36:39]\n\t"
    "v_mfma_f32_16x16x32_bf16 v[36:39], a[176:179], v[20:23], v[36:39]\n\t"
    "s_waitcnt lgkmcnt(4)\n\t"
    "s_add_u32 m0, %[ldsw], 0\n\t"
    "s_add_u32 s90, s35, 49152\n\t"
    "v_mfma_f32_16x16x32_bf16 v[40:43], a[200:203], v[8:11], 0\n\t"
    "v_cndmask_b32_e64 v56, v56, v32, s[84:85]\n\t"
    "buffer_load_dwordx4 v7, s[52:55], s90 offen nt lds\n\t"
    "v_mfma_f32_16x16x32_bf16 v[40:43], a[204:207], v[12:15], v[40:43]\n\t"
    "v_cndmask_b32_e64 v57, v57, v33, s[84:85]\n\t"
    "v_xor_b32 v6, 16, v7\n\t"
    "v_mfma_f32_16x16x32_bf16 v[40:43], a[192:195], v[16:19], v[40:43]\n\t"
    "v_cndmask_b32_e64 v58, v58, v34, s[84:85]\n\t"
    "buffer_load_dwordx4 v6, s[52:55], s90 offen offset:1024 nt lds\n\t"
    "v_mfma_f32_16x16x32_bf16 v[40:43], a[196:199], v[20:23], v[40:43]\n\t"
    "v_cndmask_b32_e64 v59, v59, v35, s[84:85]\n\t"
    "v_xor_b32 v6, 32, v7\n\t"
    "v_mfma_f32_16x16x32_bf16 v[32:35], a[216:219], v[8:11], 0\n\t"
    "v_cndmask_b32_e64 v60, v60, v36, s[84:85]\n\t"
    "buffer_load_dwordx4 v6, s[52:55], s90 offen offset:2048 nt lds\n\t"
    "v_mfma_f32_16x16x32_bf16 v[32:35], a[220:223], v[12:15], v[32:35]\n\t"
    "v_cndmask_b32_e64 v61, v61, v37, s[84:85]\n\t"
    "v_xor_b32 v6, 48, v7\n\t"
    "v_mfma_f32_16x16x32_bf16 v[32:35], a[208:211], v[16:19], v[32:35]\n\t"
    "v_cndmask_b32_e64 v62, v62, v38, s[84:85]\n\t"
    "buffer_load_dwordx4 v6, s[52:55], s90 offen offset:3072 nt lds\n\t"
    "v_mfma_f32_16x16x32_bf16 v[32:35], a[212:215], v[20:23], v[32:35]\n\t"
    "v_cndmask_b32_e64 v63, v63, v39, s[84:85]\n\t"
    "s_waitcnt lgkmcnt(0)\n\t"
    "s_add_u32 m0, %[ldsw], 4096\n\t"
    "v_mfma_f32_16x16x32_bf16 v[36:39], a[236:239], v[8:11], 0\n\t"
    "v_cndmask_b32_e64 v56, v56, v40, s[86:87]\n\t"
    "s_add_u32 s90, s35, 65536\n\t"
    "v_mfma_f32_16x16x32_bf16 v[36:39], a[224:227], v[12:15], v[36:39]\n\t"
    "v_cndmask_b32_e64 v57, v57, v41, s[86:87]\n\t"
    "buffer_load_dwordx4 v7, s[52:55], s90 offen nt lds\n\t"
    "v_mfma_f32_16x16x32_bf16 v[36:39], a[228:231], v[16:19], v[36:39]\n\t"
    "v_cndmask_b32_e64 v58, v58, v42, s[86:87]\n\t"
    "v_xor_b32 v6, 16, v7\n\t"
    "v_mfma_f32_16x16x32_bf16 v[36:39], a[232:235], v[20:23], v[36:39]\n\t"
    "v_cndmask_b32_e64 v59, v59, v43, s[86:87]\n\t"
    "buffer_load_dwordx4 v6, s[52:55], s90 offen offset:1024 nt lds\n\t"
    "v_mfma_f32_16x16x32_bf16 v[40:43], a[252:255], v[8:11], 0\n\t"
    "v_cndmask_b32_e64 v60, v60, v32, s[86:87]\n\t"
    "v_xor_b32 v6, 32, v7\n\t"
    "v_mfma_f32_16x16x32_bf16 v[40:43], a[240:243], v[12:15], v[40:43]\n\t"
    "v_cndmask_b32_e64 v61, v61, v33, s[86:87]\n\t"
    "buffer_load_dwordx4 v6, s[52:55], s90 offen offset:2048 nt lds\n\t"
    "v_mfma_f32_16x16x32_bf16 v[40:43], a[244:247], v[16:19], v[40:43]\n\t"
    "v_cndmask_b32_e64 v62, v62, v34, s[86:87]\n\t"
    "v_xor_b32 v6, 48, v7\n\t"
    "v_mfma_f32_16x16x32_bf16 v[40:43], a[248:251], v[20:23], v[40:43]\n\t"
    "v_cndmask_b32_e64 v63, v63, v35, s[86:87]\n\t"
    "buffer_load_dwordx4 v6, s[52:55], s90 offen offset:3072 nt lds\n\t"
    "s_branch 51b\n\t"
    "52:\n\t"
    "v_mbcnt_lo_u32_b32 v56, -1, 0\n\t"
    "v_mbcnt_hi_u32_b32 v56, -1, v56\n\t"
    "v_lshrrev_b32 v58, 4, v56\n\t"
    "v_xor_b32 v58, v58, v56\n\t"
    "v_and_b32 v58, 3, v58\n\t"
    "v_lshlrev_b32 v57, 6, v58\n\t"
    "v_bfe_u32 v58, v56, 2, 2\n\t"
    "v_lshl_add_u32 v57, v58, 4, v57\n\t"
    "v_and_b32 v58, 15, v56\n\t"
    "v_lshl_add_u32 v57, v58, 8, v57\n\t"
    "v_add_u32 v57, %[ldsw], v57\n\t"
    "s_waitcnt vmcnt(8)\n\t"
    "ds_read_b128 a[160:163], v57 offset:8192\n\t"
    "v_xor_b32 v58, 16, v57\n\t"
    "ds_read_b128 a[164:167], v58 offset:8192\n\t"
    "v_xor_b32 v59, 32, v57\n\t"
    "ds_read_b128 a[168:171], v59 offset:8192\n\t"
    "v_xor_b32 v58, 48, v57\n\t"
    "ds_read_b128 a[172:175], v58 offset:8192\n\t"
    "v_lshrrev_b32 v58, 4, v56\n\t"
    "v_lshlrev_b32 v57, 2, v58\n\t"
    "v_xor_b32 v57, v57, v56\n\t"
    "v_and_b32 v57, 15, v57\n\t"
    "v_lshlrev_b32 v57, 4, v57\n\t"
    "v_lshl_add_u32 v57, v58, 8, v57\n\t"
    "s_waitcnt lgkmcnt(0)\n\t"
    "s_add_u32 m0, %[ldsw], 8192\n\t"
    "s_add_u32 s90, s35, 81920\n\t"
    "buffer_load_dwordx4 v57, s[52:55], s90 offen nt lds\n\t"
    "v_xor_b32 v58, 16, v57\n\t"
    "buffer_load_dwordx4 v58, s[52:55], s90 offen offset:1024 nt lds\n\t"
    "v_xor_b32 v58, 32, v57\n\t"
    "buffer_load_dwordx4 v58, s[52:55], s90 offen offset:2048 nt lds\n\t"
    "v_xor_b32 v58, 48, v57\n\t"
    "buffer_load_dwordx4 v58, s[52:55], s90 offen offset:3072 nt lds\n\t"
    "s_branch 53b\n\t"
    "55:\n\t"
    "ds_write_b64 v28, v[60:61]\n\t"
    "s_branch 70f\n\t"
    "20:\n\t"
    "s_bitset1_b32 s75, 31\n\t"
    "buffer_load_dwordx4 v[8:11], v1, %[rws], s29 offen sc1\n\t"
    "s_branch 11b\n\t"
    "21:\n\t"
    "s_add_u32 s92, s92, 1\n\t"
    "s_cmp_gt_u32 s92, 16384\n\t"
    "s_cbranch_scc1 2b\n\t"
    "s_waitcnt lgkmcnt(0)\n\t"
    "s_add_u32 s82, s82, " STR1(GTICK) "\n\t"
    "97:\n\t"
    "s_memrealtime s[90:91]\n\t"
    "s_waitcnt lgkmcnt(0)\n\t"
    "s_sub_u32 s90, s90, s82\n\t"
    "s_cmp_lt_i32 s90, 0\n\t"
    "s_cbranch_scc1 97b\n\t"
    "s_branch 20b\n\t"
    "10:\n\t"
    "v_lshrrev_b32 v17, 4, v0\n\t"
    "v_xor_b32 v17, v17, v0\n\t"
    "v_and_b32 v17, 3, v17\n\t"
    "v_lshlrev_b32 v6, 6, v17\n\t"
    "v_bfe_u32 v17, v0, 2, 2\n\t"
    "v_lshl_add_u32 v6, v17, 4, v6\n\t"
    "v_and_b32 v17, 15, v0\n\t"
    "v_lshl_add_u32 v6, v17, 8, v6\n\t"
    "v_add_u32 v6, %[ldsw], v6\n\t"
    "v_lshrrev_b32 v17, 4, v0\n\t"
    "v_lshlrev_b32 v7, 2, v17\n\t"
    "v_xor_b32 v7, v7, v0\n\t"
    "v_and_b32 v7, 15, v7\n\t"
    "v_lshlrev_b32 v7, 4, v7\n\t"
    "v_lshl_add_u32 v7, v17, 8, v7\n\t"
    "s_bitcmp1_b32 s25, 3\n\t"
    "s_cbranch_scc0 11b\n\t"
    "s_add_u32 m0, %[ldsw], 8192\n\t"
    "s_add_u32 s90, s35, 32768\n\t"
    "buffer_load_dwordx4 v7, s[52:55], s90 offen nt lds\n\t"
    "v_xor_b32 v17, 16, v7\n\t"
    "buffer_load_dwordx4 v17, s[52:55], s90 offen offset:1024 nt lds\n\t"
    "v_xor_b32 v17, 32, v7\n\t"
    "buffer_load_dwordx4 v17, s[52:55], s90 offen offset:2048 nt lds\n\t"
    "v_xor_b32 v17, 48, v7\n\t"
    "buffer_load_dwordx4 v17, s[52:55], s90 offen offset:3072 nt lds\n\t"
    "s_waitcnt vmcnt(4)\n\t"
    "s_branch 1b\n\t"
    "15:\n\t"
    "s_sub_u32 s82, s38, s39\n\t"
    "s_and_b32 s90, s24, 1\n\t"
    "s_lshl_b32 s90, s90, 6\n\t"
    "s_bitcmp1_b32 %[outw], 11\n\t"
    "s_cselect_b32 s91, 96, 0\n\t"
    "s_lshl4_add_u32 s91, s24, s91\n\t"
    "s_load_dwordx2 s[68:69], %[ka], s90 offset:24\n\t"
    "s_load_dwordx2 s[74:75], %[ka], s91 offset:168\n\t"
    "s_sub_u32 s90, %[clus], %[mine]\n\t"
    "s_add_u32 s42, s31, s90\n\t"
    "s_add_u32 s43, s32, s90\n\t"
    "s_lshl_b32 s90, %[w], 8\n\t"
    "s_sub_u32 s90, s31, s90\n\t"
    "s_add_u32 s44, s90, 1536\n\t"
    "s_and_b32 s90, s24, 6\n\t"
    "s_cmp_eq_u32 s90, 2\n\t"
    "s_cselect_b32 s40, 3584, 0\n\t"
    "s_cmp_lt_u32 s24, 4\n\t"
    "s_cselect_b32 s90, 12288, 0\n\t"
    "s_add_u32 s80, %[ldsw], s90\n\t"
    "s_cmp_gt_u32 s24, 2\n\t"
    "s_cselect_b32 s93, 1, 0\n\t"
    "s_cselect_b32 s46, 4, 68\n\t"
    "s_cmp_eq_u32 s24, 2\n\t"
    "s_cselect_b32 s46, 20, s46\n\t"
    "s_add_u32 s81, s24, 1\n\t"
    "s_lshl2_add_u32 s90, s93, %[w]\n\t"
    "s_lshl_b32 s90, s90, 9\n\t"
    "s_add_u32 s50, s90, 159552\n\t"
    "s_lshl1_add_u32 s90, s93, s24\n\t"
    "s_mul_i32 s90, s90, 1792\n\t"
    "s_add_u32 s51, s90, %[ybase]\n\t"
    "s_add_u32 s90, s90, %[mine]\n\t"
    "s_add_u32 s76, s90, 1664\n\t"
    "s_mov_b32 s47, 157952\n\t"
    "s_mul_i32 s48, %[w], 384\n\t"
    "s_add_u32 s48, s48, s47\n\t"
    "s_lshl4_add_u32 s49, %[w], 159488\n\t"
    "s_bitcmp1_b32 %[outw], 11\n\t"
    "s_cbranch_scc0 80f\n\t"
    "60:\n\t"
    "v_mbcnt_lo_u32_b32 v50, -1, 0\n\t"
    "v_mbcnt_hi_u32_b32 v50, -1, v50\n\t"
    "v_lshrrev_b32 v48, 4, v50\n\t"
    "v_xor_b32 v48, v48, v50\n\t"
    "v_and_b32 v48, 3, v48\n\t"
    "v_lshlrev_b32 v45, 6, v48\n\t"
    "v_bfe_u32 v48, v50, 2, 2\n\t"
    "v_lshl_add_u32 v45, v48, 4, v45\n\t"
    "v_and_b32 v48, 15, v50\n\t"
    "v_lshl_add_u32 v45, v48, 8, v45\n\t"
    "v_add_u32 v45, %[ldsw], v45\n\t"
    "v_lshrrev_b32 v48, 4, v50\n\t"
    "v_lshlrev_b32 v44, 2, v48\n\t"
    "v_xor_b32 v44, v44, v50\n\t"
    "v_and_b32 v44, 15, v44\n\t"
    "v_lshlrev_b32 v44, 4, v44\n\t"
    "v_lshl_add_u32 v44, v48, 8, v44\n\t"
    "s_waitcnt vmcnt(8)\n\t"
    TP(23)
    "ds_read_b128 a[48:51], v45 offset:0\n\t"
    "v_xor_b32 v48, 16, v45\n\t"
    "ds_read_b128 a[52:55], v48 offset:0\n\t"
    "v_xor_b32 v49, 32, v45\n\t"
    "ds_read_b128 a[56:59], v49 offset:0\n\t"
    "v_xor_b32 v48, 48, v45\n\t"
    "ds_read_b128 a[60:63], v48 offset:0\n\t"
    "s_waitcnt lgkmcnt(0)\n\t"
    "s_add_u32 m0, %[ldsw], 0\n\t"
    "s_add_u32 s90, s35, 98304\n\t"
    "buffer_load_dwordx4 v44, s[52:55], s90 offen nt lds\n\t"
    "v_xor_b32 v48, 16, v44\n\t"
    "buffer_load_dwordx4 v48, s[52:55], s90 offen offset:1024 nt lds\n\t"
    "v_xor_b32 v48, 32, v44\n\t"
    "buffer_load_dwordx4 v48, s[52:55], s90 offen offset:2048 nt lds\n\t"
    "v_xor_b32 v48, 48, v44\n\t"
    "buffer_load_dwordx4 v48, s[52:55], s90 offen offset:3072 nt lds\n\t"
    "v_lshrrev_b32 v52, 4, v50\n\t"
    "v_lshlrev_b32 v47, 1, v52\n\t"
    "v_xor_b32 v47, v47, v50\n\t"
    "v_and_b32 v47, 15, v47\n\t"
    "v_lshlrev_b32 v47, 4, v47\n\t"
    "v_lshl_add_u32 v47, v52, 8, v47\n\t"
    "v_lshrrev_b32 v52, 2, v50\n\t"
    "v_lshlrev_b32 v31, 8, v52\n\t"
    "v_and_b32 v52, 7, v52\n\t"
    "v_lshl_add_u32 v31, v52, 5, v31\n\t"
    "v_and_b32 v52, 3, v50\n\t"
    "v_lshl_add_u32 v31, v52, 3, v31\n\t"
    "v_add_u32 v31, %[ldsw], v31\n\t"
    "v_and_b32 v51, 3, v50\n\t"
    "v_lshrrev_b32 v52, 4, v50\n\t"
    "v_lshl_add_u32 v51, v51, 2, v52\n\t"
    "v_mul_u32_u24 v51, 14592, v51\n\t"
    "v_and_b32 v52, 12, v50\n\t"
    "v_lshl_add_u32 v51, v52, 2, v51\n\t"
    "s_mov_b32 m0, s77\n\t"
    "s_lshl_b32 exec_lo, 0x11111111, %[w]\n\t"
    "s_mov_b32 exec_hi, exec_lo\n\t"
    "buffer_load_dwordx4 v51, %[rws], s30 offen sc1 lds\n\t"
    "s_mov_b64 exec, -1\n\t"
    "s_waitcnt vmcnt(9)\n\t"
    TP(24)
    "ds_read_b128 a[64:67], v45 offset:4096\n\t"
    "v_xor_b32 v48, 16, v45\n\t"
    "ds_read_b128 a[68:71], v48 offset:4096\n\t"
    "v_xor_b32 v49, 32, v45\n\t"
    "ds_read_b128 a[72:75], v49 offset:4096\n\t"
    "v_xor_b32 v48, 48, v45\n\t"
    "ds_read_b128 a[76:79], v48 offset:4096\n\t"
    "s_waitcnt lgkmcnt(0)\n\t"
    "s_add_u32 m0, %[ldsw], 4096\n\t"
    "s_add_u32 s90, s35, 114688\n\t"
    "buffer_load_dwordx4 v44, s[52:55], s90 offen nt lds\n\t"
    "v_xor_b32 v48, 16, v44\n\t"
    "buffer_load_dwordx4 v48, s[52:55], s90 offen offset:1024 nt lds\n\t"
    "v_xor_b32 v48, 32, v44\n\t"
    "buffer_load_dwordx4 v48, s[52:55], s90 offen offset:2048 nt lds\n\t"
    "v_xor_b32 v48, 48, v44\n\t"
    "buffer_load_dwordx4 v48, s[52:55], s90 offen offset:3072 nt lds\n\t"
    "v_and_b32 v5, 3, v50\n\t"
    "v_mul_u32_u24 v5, 528, v5\n\t"
    "v_add_u32 v5, s26, v5\n\t"
    "v_and_b32 v52, 12, v50\n\t"
    "v_lshl_add_u32 v4, v52, 4, v5\n\t"
    "v_and_b32 v52, 48, v50\n\t"
    "v_add_u32 v4, v4, v52\n\t"
    "v_lshl_add_u32 v28, v50, 3, s27\n\t"
    "v_lshlrev_b32 v30, 24, v50\n\t"
    "v_lshlrev_b32 v29, 2, v50\n\t"
    "s_mov_b32 s84, 0xf000f0\n\t"
    "s_mov_b32 s85, 0xf000f0\n\t"
    "s_mov_b32 s86, 0xf000f00\n\t"
    "s_mov_b32 s87, 0xf000f00\n\t"
    "s_mov_b32 s88, 0xf000f000\n\t"
    "s_mov_b32 s89, 0xf000f000\n\t"
    "v_mov_b32 v0, 0x3f803f80\n\t"
    "v_mov_b32 v1, 0x3f803f80\n\t"
    "v_mov_b32 v2, 0x3f803f80\n\t"
    "v_mov_b32 v3, 0x3f803f80\n\t"
    "s_mov_b32 s92, 0\n\t"
    "s_add_u32 s38, s38, s72\n\t"
    "s_add_u32 s90, s82, s39\n\t"
    "94:\n\t"
    "s_sub_u32 s91, s90, s38\n\t"
    "s_cmp_gt_i32 s91, 0\n\t"
    "s_cbranch_scc0 93f\n\t"
    "s_add_u32 s38, s38, " STR1(GTICK) "\n\t"
    "s_branch 94b\n\t"
    "93:\n\t"
    "s_waitcnt vmcnt(9)\n\t"
    TP(25)
    "ds_read_b128 a[80:83], v45 offset:8192\n\t"
    "v_xor_b32 v48, 16, v45\n\t"
    "ds_read_b128 a[84:87], v48 offset:8192\n\t"
    "v_xor_b32 v49, 32, v45\n\t"
    "ds_read_b128 a[88:91], v49 offset:8192\n\t"
    "v_xor_b32 v48, 48, v45\n\t"
    "ds_read_b128 a[92:95], v48 offset:8192\n\t"
    "s_waitcnt lgkmcnt(0)\n\t"
    "s_add_u32 m0, %[ldsw], 20480\n\t"
    "s_add_u32 s90, s35, 0\n\t"
    "v_xor_b32 v48, 128, v47\n\t"
    "buffer_load_dwordx4 v47, s[56:59], s90 offen offset:0 nt lds\n\t"
    "buffer_load_dwordx4 v48, s[56:59], s90 offen offset:1024 nt lds\n\t"
    "buffer_load_dwordx4 v47, s[56:59], s90 offen offset:2048 nt lds\n\t"
    "buffer_load_dwordx4 v48, s[56:59], s90 offen offset:3072 nt lds\n\t"
    "s_waitcnt vmcnt(8)\n\t"
    TP(26)
    "ds_read_b128 a[96:99], v45 offset:0\n\t"
    "v_xor_b32 v48, 16, v45\n\t"
    "ds_read_b128 a[100:103], v48 offset:0\n\t"
    "v_xor_b32 v49, 32, v45\n\t"
    "ds_read_b128 a[104:107], v49 offset:0\n\t"
    "v_xor_b32 v48, 48, v45\n\t"
    "ds_read_b128 a[108:111], v48 offset:0\n\t"
    "s_waitcnt lgkmcnt(0)\n\t"
    "s_add_u32 m0, %[ldsw], 24576\n\t"
    "s_add_u32 s90, s35, 16384\n\t"
    "v_xor_b32 v48, 128, v47\n\t"
    "buffer_load_dwordx4 v47, s[56:59], s90 offen offset:0 nt lds\n\t"
    "buffer_load_dwordx4 v48, s[56:59], s90 offen offset:1024 nt lds\n\t"
    "buffer_load_dwordx4 v47, s[56:59], s90 offen offset:2048 nt lds\n\t"
    "buffer_load_dwordx4 v48, s[56:59], s90 offen offset:3072 nt lds\n\t"
    "v_lshl_add_u32 v52, v50, 4, s77\n\t"
    "s_barrier\n\t"
    "ds_read_b128 v[8:11], v52\n\t"
    "s_waitcnt lgkmcnt(0)\n\t"
    "62:\n\t"
    "v_max3_u32 v52, v8, v9, v10\n\t"
    "v_max_u32 v52, v52, v11\n\t"
    "v_cmp_eq_u32 vcc, -1, v52\n\t"
    "s_cbranch_vccnz 64f\n\t"
    "63:\n\t"
    "v_mov_b32_dpp v12, v8 row_ror:12 row_mask:0xf bank_mask:0xf\n\t"
    "v_mov_b32_dpp v13, v9 row_ror:12 row_mask:0xf bank_mask:0xf\n\t"
    "v_mov_b32_dpp v14, v10 row_ror:12 row_mask:0xf bank_mask:0xf\n\t"
    "v_mov_b32_dpp v15, v11 row_ror:12 row_mask:0xf bank_mask:0xf\n\t"
    "v_mov_b32_dpp v16, v8 row_ror:8 row_mask:0xf bank_mask:0xf\n\t"
    "v_mov_b32_dpp v17, v9 row_ror:8 row_mask:0xf bank_mask:0xf\n\t"
    "v_mov_b32_dpp v18, v10 row_ror:8 row_mask:0xf bank_mask:0xf\n\t"
    "v_mov_b32_dpp v19, v11 row_ror:8 row_mask:0xf bank_mask:0xf\n\t"
    "v_mov_b32_dpp v20, v8 row_ror:4 row_mask:0xf bank_mask:0xf\n\t"
    "v_mov_b32_dpp v21, v9 row_ror:4 row_mask:0xf bank_mask:0xf\n\t"
    "v_mov_b32_dpp v22, v10 row_ror:4 row_mask:0xf bank_mask:0xf\n\t"
    "v_mov_b32_dpp v23, v11 row_ror:4 row_mask:0xf bank_mask:0xf\n\t"
    "s_nop 1\n\t"
    "v_mfma_f32_16x16x32_bf16 v[56:59], a[0:3], v[8:11], 0\n\t"
    "v_mfma_f32_16x16x32_bf16 v[56:59], a[4:7], v[12:15], v[56:59]\n\t"
    "v_mfma_f32_16x16x32_bf16 v[56:59], a[8:11], v[16:19], v[56:59]\n\t"
    "v_mfma_f32_16x16x32_bf16 v[56:59], a[12:15], v[20:23], v[56:59]\n\t"
    "v_mfma_f32_16x16x32_bf16 v[60:63], a[16:19], v[8:11], 0\n\t"
    "v_mfma_f32_16x16x32_bf16 v[60:63], a[20:23], v[12:15], v[60:63]\n\t"
    "v_mfma_f32_16x16x32_bf16 v[60:63], a[24:27], v[16:19], v[60:63]\n\t"
    "v_mfma_f32_16x16x32_bf16 v[60:63], a[28:31], v[20:23], v[60:63]\n\t"
    "v_mfma_f32_16x16x32_bf16 v[32:35], a[36:39], v[8:11], 0\n\t"
    "v_mfma_f32_16x16x32_bf16 v[32:35], a[40:43], v[12:15], v[32:35]\n\t"
    "v_mfma_f32_16x16x32_bf16 v[32:35], a[44:47], v[16:19], v[32:35]\n\t"
    "v_mfma_f32_16x16x32_bf16 v[32:35], a[32:35], v[20:23], v[32:35]\n\t"
    "s_waitcnt vmcnt(8)\n\t"
    TP(27)
    "ds_read_b128 a[112:115], v45 offset:4096\n\t"
    "v_xor_b32 v48, 16, v45\n\t"
    "ds_read_b128 a[116:119], v48 offset:4096\n\t"
    "v_xor_b32 v49, 32, v45\n\t"
    "ds_read_b128 a[120:123], v49 offset:4096\n\t"
    "v_xor_b32 v48, 48, v45\n\t"
    "ds_read_b128 a[124:127], v48 offset:4096\n\t"
    "s_waitcnt lgkmcnt(0)\n\t"
    "s_add_u32 m0, %[ldsw], 0\n\t"
    "s_add_u32 s90, s35, 32768\n\t"
    "v_xor_b32 v48, 128, v47\n\t"
    "buffer_load_dwordx4 v47, s[56:59], s90 offen offset:0 nt lds\n\t"
    "buffer_load_dwordx4 v48, s[56:59], s90 offen offset:1024 nt lds\n\t"
    "buffer_load_dwordx4 v47, s[56:59], s90 offen offset:2048 nt lds\n\t"
    "buffer_load_dwordx4 v48, s[56:59], s90 offen offset:3072 nt lds\n\t"
    "v_mfma_f32_16x16x32_bf16 v[36:39], a[52:55], v[8:11], 0\n\t"
    "s_nop 0\n\t"
    "v_mfma_f32_16x16x32_bf16 v[36:39], a[56:59], v[12:15], v[36:39]\n\t"
    "s_nop 0\n\t"
    "v_mfma_f32_16x16x32_bf16 v[36:39], a[60:63], v[16:19], v[36:39]\n\t"
    "s_nop 0\n\t"
    "v_mfma_f32_16x16x32_bf16 v[36:39], a[48:51], v[20:23], v[36:39]\n\t"
    "s_nop 0\n\t"
    "v_mfma_f32_16x16x32_bf16 v[40:43], a[72:75], v[8:11], 0\n\t"
    "v_cndmask_b32_e64 v56, v56, v32, s[84:85]\n\t"
    "v_mfma_f32_16x16x32_bf16 v[40:43], a[76:79], v[12:15], v[40:43]\n\t"
    "v_cndmask_b32_e64 v57, v57, v33, s[84:85]\n\t"
    "v_mfma_f32_16x16x32_bf16 v[40:43], a[64:67], v[16:19], v[40:43]\n\t"
    "v_cndmask_b32_e64 v58, v58, v34, s[84:85]\n\t"
    "v_mfma_f32_16x16x32_bf16 v[40:43], a[68:71], v[20:23], v[40:43]\n\t"
    "v_cndmask_b32_e64 v59, v59, v35, s[84:85]\n\t"
    "v_mfma_f32_16x16x32_bf16 v[32:35], a[88:91], v[8:11], 0\n\t"
    "v_cndmask_b32_e64 v60, v60, v36, s[84:85]\n\t"
    "v_mfma_f32_16x16x32_bf16 v[32:35], a[92:95], v[12:15], v[32:35]\n\t"
    "v_cndmask_b32_e64 v61, v61, v37, s[84:85]\n\t"
    "v_mfma_f32_16x16x32_bf16 v[32:35], a[80:83], v[16:19], v[32:35]\n\t"
    "v_cndmask_b32_e64 v62, v62, v38, s[84:85]\n\t"
    "v_mfma_f32_16x16x32_bf16 v[32:35], a[84:87], v[20:23], v[32:35]\n\t"
    "v_cndmask_b32_e64 v63, v63, v39, s[84:85]\n\t"
    "v_mfma_f32_16x16x32_bf16 v[36:39], a[108:111], v[8:11], 0\n\t"
    "v_cndmask_b32_e64 v56, v56, v40, s[86:87]\n\t"
    "v_mfma_f32_16x16x32_bf16 v[36:39], a[96:99], v[12:15], v[36:39]\n\t"
    "v_cndmask_b32_e64 v57, v57, v41, s[86:87]\n\t"
    "v_mfma_f32_16x16x32_bf16 v[36:39], a[100:103], v[16:19], v[36:39]\n\t"
    "v_cndmask_b32_e64 v58, v58, v42, s[86:87]\n\t"
    "v_mfma_f32_16x16x32_bf16 v[36:39], a[104:107], v[20:23], v[36:39]\n\t"
    "v_cndmask_b32_e64 v59, v59, v43, s[86:87]\n\t"
    "v_mfma_f32_16x16x32_bf16 v[40:43], a[124:127], v[8:11], 0\n\t"
    "v_cndmask_b32_e64 v60, v60, v32, s[86:87]\n\t"
    "v_mfma_f32_16x16x32_bf16 v[40:43], a[112:115], v[12:15], v[40:43]\n\t"
    "v_cndmask_b32_e64 v61, v61, v33, s[86:87]\n\t"
    "v_mfma_f32_16x16x32_bf16 v[40:43], a[116:119], v[16:19], v[40:43]\n\t"
    "v_cndmask_b32_e64 v62, v62, v34, s[86:87]\n\t"
    "v_mfma_f32_16x16x32_bf16 v[40:43], a[120:123], v[20:23], v[40:43]\n\t"
    "v_cndmask_b32_e64 v63, v63, v35, s[86:87]\n\t"
    "s_waitcnt vmcnt(8)\n\t"
    TP(28)
    "s_add_u32 m0, %[ldsw], 4096\n\t"
    "s_add_u32 s90, s35, 49152\n\t"
    "v_xor_b32 v48, 128, v47\n\t"
    "buffer_load_dwordx4 v47, s[56:59], s90 offen offset:0 nt lds\n\t"
    "buffer_load_dwordx4 v48, s[56:59], s90 offen offset:1024 nt lds\n\t"
    "buffer_load_dwordx4 v47, s[56:59], s90 offen offset:2048 nt lds\n\t"
    "buffer_load_dwordx4 v48, s[56:59], s90 offen offset:3072 nt lds\n\t"
    "v_cndmask_b32_e64 v56, v56, v36, s[88:89]\n\t"
    "v_cndmask_b32_e64 v57, v57, v37, s[88:89]\n\t"
    "v_cndmask_b32_e64 v58, v58, v38, s[88:89]\n\t"
    "v_cndmask_b32_e64 v59, v59, v39, s[88:89]\n\t"
    "v_max3_f32 v6, v56, v57, v58\n\t"
    "v_max_f32 v6, v6, v59\n\t"
    "s_nop 1\n\t"
    "v_cndmask_b32_e64 v60, v60, v40, s[88:89]\n\t"
    "v_cndmask_b32_e64 v61, v61, v41, s[88:89]\n\t"
    "v_cndmask_b32_e64 v62, v62, v42, s[88:89]\n\t"
    "v_cndmask_b32_e64 v63, v63, v43, s[88:89]\n\t"
    "v_max3_f32 v6, v6, v60, v61\n\t"
    "v_max3_f32 v6, v6, v62, v63\n\t"
    "s_nop 1\n\t"
    "v_max_f32_dpp v6, v6, v6 row_ror:4 row_mask:0xf bank_mask:0xf\n\t"
    "s_nop 1\n\t"
    "v_max_f32_dpp v6, v6, v6 row_ror:8 row_mask:0xf bank_mask:0xf\n\t"
    "v_mov_b32 v46, v6\n\t"
    "s_nop 1\n\t"
    "v_permlane16_swap_b32 v6, v46\n\t"
    "v_max_f32 v6, v6, v46\n\t"
    "v_mov_b32 v46, v6\n\t"
    "s_nop 1\n\t"
    "v_permlane32_swap_b32 v6, v46\n\t"
    "v_max_f32 v6, v6, v46\n\t"
    "v_mul_f32 v6, s28, v6\n\t"
    "v_fma_f32 v56, v56, s28, -v6\n\t"
    "v_fma_f32 v57, v57, s28, -v6\n\t"
    "v_fma_f32 v58, v58, s28, -v6\n\t"
    "v_fma_f32 v59, v59, s28, -v6\n\t"
    "v_fma_f32 v60, v60, s28, -v6\n\t"
    "v_fma_f32 v61, v61, s28, -v6\n\t"
    "v_fma_f32 v62, v62, s28, -v6\n\t"
    "v_fma_f32 v63, v63, s28, -v6\n\t"
    "v_exp_f32 v56, v56\n\t"
    "v_exp_f32 v57, v57\n\t"
    "v_exp_f32 v58, v58\n\t"
    "v_exp_f32 v59, v59\n\t"
    "v_exp_f32 v60, v60\n\t"
    "v_exp_f32 v61, v61\n\t"
    "v_exp_f32 v62, v62\n\t"
    "v_exp_f32 v63, v63\n\t"
    "v_cvt_pk_bf16_f32 v8, v56, v57\n\t"
    "v_cvt_pk_bf16_f32 v9, v58, v59\n\t"
    "v_cvt_pk_bf16_f32 v10, v60, v61\n\t"
    "v_cvt_pk_bf16_f32 v11, v62, v63\n\t"
    "s_nop 1\n\t"
    "v_mfma_f32_16x16x32_bf16 v[56:59], v[0:3], v[8:11], 0\n\t"
    "v_mov_b32_dpp v12, v8 row_ror:12 row_mask:0xf bank_mask:0xf\n\t"
    "v_mov_b32_dpp v13, v9 row_ror:12 row_mask:0xf bank_mask:0xf\n\t"
    "v_mov_b32_dpp v14, v10 row_ror:12 row_mask:0xf bank_mask:0xf\n\t"
    "v_mov_b32_dpp v15, v11 row_ror:12 row_mask:0xf bank_mask:0xf\n\t"
    "v_mov_b32_dpp v16, v8 row_ror:8 row_mask:0xf bank_mask:0xf\n\t"
    "v_mov_b32_dpp v17, v9 row_ror:8 row_mask:0xf bank_mask:0xf\n\t"
    "v_mov_b32_dpp v18, v10 row_ror:8 row_mask:0xf bank_mask:0xf\n\t"
    "v_mov_b32_dpp v19, v11 row_ror:8 row_mask:0xf bank_mask:0xf\n\t"
    "v_add_f32_dpp v7, v56, v56 row_ror:4 row_mask:0xf bank_mask:0xf\n\t"
    "v_mov_b32 v57, v47\n\t"
    "s_nop 0\n\t"
    "v_add_f32_dpp v7, v7, v7 row_ror:8 row_mask:0xf bank_mask:0xf\n\t"
    "ds_write_b64 v5, v[6:7] offset:512\n\t"
    "s_waitcnt vmcnt(8)\n\t"
    TP(29)
    "s_add_u32 m0, %[ldsw], 8192\n\t"
    "s_add_u32 s90, s35, 65536\n\t"
    "v_xor_b32 v58, 128, v57\n\t"
    "buffer_load_dwordx4 v57, s[56:59], s90 offen offset:0 nt lds\n\t"
    "buffer_load_dwordx4 v58, s[56:59], s90 offen offset:1024 nt lds\n\t"
    "buffer_load_dwordx4 v57, s[56:59], s90 offen offset:2048 nt lds\n\t"
    "buffer_load_dwordx4 v58, s[56:59], s90 offen offset:3072 nt lds\n\t"
    "ds_read_b64_tr_b16 v[32:33], v31 offset:20480\n\t"
    "ds_read_b64_tr_b16 v[34:35], v31 offset:24576\n\t"
    "v_xor_b32 v59, 32, v31\n\t"
    "ds_read_b64_tr_b16 v[36:37], v59 offset:20480\n\t"
    "ds_read_b64_tr_b16 v[38:39], v59 offset:24576\n\t"
    "v_xor_b32 v58, 64, v31\n\t"
    "ds_read_b64_tr_b16 v[40:41], v58 offset:20480\n\t"
    "ds_read_b64_tr_b16 v[42:43], v58 offset:24576\n\t"
    "v_xor_b32 v59, 96, v31\n\t"
    "ds_read_b64_tr_b16 v[44:45], v59 offset:20480\n\t"
    "ds_read_b64_tr_b16 v[46:47], v59 offset:24576\n\t"
    "v_xor_b32 v58, 128, v31\n\t"
    "ds_read_b64_tr_b16 v[48:49], v58 offset:20480\n\t"
    "ds_read_b64_tr_b16 v[50:51], v58 offset:24576\n\t"
    "v_xor_b32 v59, 160, v31\n\t"
    "ds_read_b64_tr_b16 v[52:53], v59 offset:20480\n\t"
    "ds_read_b64_tr_b16 v[54:55], v59 offset:24576\n\t"
    "v_xor_b32 v58, 192, v31\n\t"
    "ds_read_b64_tr_b16 v[20:21], v58 offset:20480\n\t"
    "ds_read_b64_tr_b16 v[22:23], v58 offset:24576\n\t"
    "v_xor_b32 v59, 224, v31\n\t"
    "ds_read_b64_tr_b16 v[24:25], v59 offset:20480\n\t"
    "ds_read_b64_tr_b16 v[26:27], v59 offset:24576\n\t"
    "s_waitcnt lgkmcnt(0)\n\t"
    "v_mfma_f32_16x16x32_bf16 v[0:3], v[32:35], v[8:11], 0\n\t"
    "v_mfma_f32_16x16x32_bf16 v[32:35], v[36:39], v[8:11], 0\n\t"
    "v_mfma_f32_16x16x32_bf16 v[36:39], v[40:43], v[8:11], 0\n\t"
    "v_mfma_f32_16x16x32_bf16 v[40:43], v[44:47], v[8:11], 0\n\t"
    "v_mfma_f32_16x16x32_bf16 v[44:47], v[48:51], v[8:11], 0\n\t"
    "v_mfma_f32_16x16x32_bf16 v[48:51], v[52:55], v[8:11], 0\n\t"
    "v_mfma_f32_16x16x32_bf16 v[52:55], v[20:23], v[8:11], 0\n\t"
    "v_mfma_f32_16x16x32_bf16 v[20:23], v[24:27], v[8:11], 0\n\t"
    "s_waitcnt vmcnt(8)\n\t"
    TP(30)
    "ds_read_b64_tr_b16 v[64:65], v31 offset:0\n\t"
    "v_xor_b32 v58, 32, v31\n\t"
    "ds_read_b64_tr_b16 v[68:69], v58 offset:0\n\t"
    "v_xor_b32 v59, 64, v31\n\t"
    "ds_read_b64_tr_b16 v[72:73], v59 offset:0\n\t"
    "v_xor_b32 v58, 96, v31\n\t"
    "ds_read_b64_tr_b16 v[76:77], v58 offset:0\n\t"
    "v_xor_b32 v59, 128, v31\n\t"
    "ds_read_b64_tr_b16 v[80:81], v59 offset:0\n\t"
    "v_xor_b32 v58, 160, v31\n\t"
    "ds_read_b64_tr_b16 v[84:85], v58 offset:0\n\t"
    "v_xor_b32 v59, 192, v31\n\t"
    "ds_read_b64_tr_b16 v[88:89], v59 offset:0\n\t"
    "v_xor_b32 v58, 224, v31\n\t"
    "ds_read_b64_tr_b16 v[92:93], v58 offset:0\n\t"
    "s_waitcnt lgkmcnt(0)\n\t"
    "s_add_u32 m0, %[ldsw], 0\n\t"
    "s_add_u32 s90, s35, 81920\n\t"
    "v_xor_b32 v58, 128, v57\n\t"
    "buffer_load_dwordx4 v57, s[56:59], s90 offen offset:0 nt lds\n\t"
    "buffer_load_dwordx4 v58, s[56:59], s90 offen offset:1024 nt lds\n\t"
    "buffer_load_dwordx4 v57, s[56:59], s90 offen offset:2048 nt lds\n\t"
    "buffer_load_dwordx4 v58, s[56:59], s90 offen offset:3072 nt lds\n\t"
    "s_waitcnt vmcnt(8)\n\t"
    TP(31)
    "ds_read_b64_tr_b16 v[66:67], v31 offset:4096\n\t"
    "v_xor_b32 v58, 32, v31\n\t"
    "ds_read_b64_tr_b16 v[70:71], v58 offset:4096\n\t"
    "v_xor_b32 v59, 64, v31\n\t"
    "ds_read_b64_tr_b16 v[74:75], v59 offset:4096\n\t"
    "v_xor_b32 v58, 96, v31\n\t"
    "ds_read_b64_tr_b16 v[78:79], v58 offset:4096\n\t"
    "v_xor_b32 v59, 128, v31\n\t"
    "ds_read_b64_tr_b16 v[82:83], v59 offset:4096\n\t"
    "v_xor_b32 v58, 160, v31\n\t"
    "ds_read_b64_tr_b16 v[86:87], v58 offset:4096\n\t"
    "v_xor_b32 v59, 192, v31\n\t"
    "ds_read_b64_tr_b16 v[90:91], v59 offset:4096\n\t"
    "v_xor_b32 v58, 224, v31\n\t"
    "ds_read_b64_tr_b16 v[94:95], v58 offset:4096\n\t"
    "s_waitcnt lgkmcnt(0)\n\t"
    "s_add_u32 m0, %[ldsw], 4096\n\t"
    "s_add_u32 s90, s35, 98304\n\t"
    "v_xor_b32 v58, 128, v57\n\t"
    "buffer_load_dwordx4 v57, s[56:59], s90 offen offset:0 nt lds\n\t"
    "buffer_load_dwordx4 v58, s[56:59], s90 offen offset:1024 nt lds\n\t"
    "buffer_load_dwordx4 v57, s[56:59], s90 offen offset:2048 nt lds\n\t"
    "buffer_load_dwordx4 v58, s[56:59], s90 offen offset:3072 nt lds\n\t"
    "v_mfma_f32_16x16x32_bf16 v[0:3], v[64:67], v[12:15], v[0:3]\n\t"
    "v_mov_b32_dpp v24, v8 row_ror:4 row_mask:0xf bank_mask:0xf\n\t"
    "v_mfma_f32_16x16x32_bf16 v[32:35], v[68:71], v[12:15], v[32:35]\n\t"
    "v_mov_b32_dpp v25, v9 row_ror:4 row_mask:0xf bank_mask:0xf\n\t"
    "v_mfma_f32_16x16x32_bf16 v[36:39], v[72:75], v[12:15], v[36:39]\n\t"
    "v_mov_b32_dpp v26, v10 row_ror:4 row_mask:0xf bank_mask:0xf\n\t"
    "v_mfma_f32_16x16x32_bf16 v[40:43], v[76:79], v[12:15], v[40:43]\n\t"
    "v_mov_b32_dpp v27, v11 row_ror:4 row_mask:0xf bank_mask:0xf\n\t"
    "v_mfma_f32_16x16x32_bf16 v[44:47], v[80:83], v[12:15], v[44:47]\n\t"
    "v_and_b32 v62, 12, v29\n\t"
    "v_mfma_f32_16x16x32_bf16 v[48:51], v[84:87], v[12:15], v[48:51]\n\t"
    "v_mul_u32_u24 v62, 528, v62\n\t"
    "v_mfma_f32_16x16x32_bf16 v[52:55], v[88:91], v[12:15], v[52:55]\n\t"
    "v_add_u32 v62, s27, v62\n\t"
    "v_mfma_f32_16x16x32_bf16 v[20:23], v[92:95], v[12:15], v[20:23]\n\t"
    "s_waitcnt vmcnt(8)\n\t"
    TP(32)
    "ds_read_b64_tr_b16 v[96:97], v31 offset:8192\n\t"
    "v_xor_b32 v58, 32, v31\n\t"
    "ds_read_b64_tr_b16 v[100:101], v58 offset:8192\n\t"
    "v_xor_b32 v59, 64, v31\n\t"
    "ds_read_b64_tr_b16 v[104:105], v59 offset:8192\n\t"
    "v_xor_b32 v58, 96, v31\n\t"
    "ds_read_b64_tr_b16 v[108:109], v58 offset:8192\n\t"
    "v_xor_b32 v59, 128, v31\n\t"
    "ds_read_b64_tr_b16 v[112:113], v59 offset:8192\n\t"
    "v_xor_b32 v58, 160, v31\n\t"
    "ds_read_b64_tr_b16 v[116:117], v58 offset:8192\n\t"
    "v_xor_b32 v59, 192, v31\n\t"
    "ds_read_b64_tr_b16 v[120:121], v59 offset:8192\n\t"
    "v_xor_b32 v58, 224, v31\n\t"
    "ds_read_b64_tr_b16 v[124:125], v58 offset:8192\n\t"
    "s_waitcnt lgkmcnt(0)\n\t"
    "s_add_u32 m0, %[ldsw], 8192\n\t"
    "s_add_u32 s90, s35, 114688\n\t"
    "v_xor_b32 v58, 128, v57\n\t"
    "buffer_load_dwordx4 v57, s[56:59], s90 offen offset:0 nt lds\n\t"
    "buffer_load_dwordx4 v58, s[56:59], s90 offen offset:1024 nt lds\n\t"
    "buffer_load_dwordx4 v57, s[56:59], s90 offen offset:2048 nt lds\n\t"
    "buffer_load_dwordx4 v58, s[56:59], s90 offen offset:3072 nt lds\n\t"
    "ds_read_b64 v[12:13], v28\n\t"
    "s_waitcnt lgkmcnt(0)\n\t"
    "s_barrier\n\t"
    "ds_read_b64 v[60:61], v62 offset:512\n\t"
    "s_waitcnt lgkmcnt(0)\n\t"
    "s_nop 1\n\t"
    "v_max_f32_dpp v62, v60, v60 quad_perm:[1,0,3,2] row_mask:0xf bank_mask:0xf\n\t"
    "s_nop 1\n\t"
    "v_max_f32_dpp v62, v62, v62 quad_perm:[2,3,0,1] row_mask:0xf bank_mask:0xf\n\t"
    "v_sub_f32 v60, v60, v62\n\t"
    "v_exp_f32 v60, v60\n\t"
    "s_nop 1\n\t"
    "v_mul_f32 v61, v60, v61\n\t"
    "s_nop 1\n\t"
    "v_add_f32_dpp v63, v61, v61 quad_perm:[1,0,3,2] row_mask:0xf bank_mask:0xf\n\t"
    "s_nop 1\n\t"
    "v_add_f32_dpp v63, v63, v63 quad_perm:[2,3,0,1] row_mask:0xf bank_mask:0xf\n\t"
    "v_log_f32 v61, v63\n\t"
    "v_rcp_f32 v63, v63\n\t"
    "s_nop 1\n\t"
    "v_add_f32 v61, v61, v62\n\t"
    "v_mul_f32 v60, v60, v63\n\t"
    "v_min_u32 v61, -2, v61\n\t"
    "s_bitcmp1_b32 s25, 4\n\t"
    "s_cselect_b64 vcc, -1, 0\n\t"
    "s_waitcnt vmcnt(8)\n\t"
    TP(33)
    "ds_read_b64_tr_b16 v[98:99], v31 offset:0\n\t"
    "v_xor_b32 v58, 32, v31\n\t"
    "ds_read_b64_tr_b16 v[102:103], v58 offset:0\n\t"
    "v_xor_b32 v59, 64, v31\n\t"
    "ds_read_b64_tr_b16 v[106:107], v59 offset:0\n\t"
    "v_xor_b32 v58, 96, v31\n\t"
    "ds_read_b64_tr_b16 v[110:111], v58 offset:0\n\t"
    "v_xor_b32 v59, 128, v31\n\t"
    "ds_read_b64_tr_b16 v[114:115], v59 offset:0\n\t"
    "v_xor_b32 v58, 160, v31\n\t"
    "ds_read_b64_tr_b16 v[118:119], v58 offset:0\n\t"
    "v_xor_b32 v59, 192, v31\n\t"
    "ds_read_b64_tr_b16 v[122:123], v59 offset:0\n\t"
    "v_xor_b32 v58, 224, v31\n\t"
    "ds_read_b64_tr_b16 v[126:127], v58 offset:0\n\t"
    "s_waitcnt lgkmcnt(0)\n\t"
    "s_cbranch_vccz 96f\n\t"
    "v_lshrrev_b32 v59, 2, v29\n\t"
    "v_lshrrev_b32 v58, 4, v59\n\t"
    "v_lshlrev_b32 v57, 2, v58\n\t"
    "v_xor_b32 v57, v57, v59\n\t"
    "v_and_b32 v57, 15, v57\n\t"
    "v_lshlrev_b32 v57, 4, v57\n\t"
    "v_lshl_add_u32 v57, v58, 8, v57\n\t"
    "s_add_u32 m0, %[ldsw], 0\n\t"
    "s_add_u32 s90, s36, 0\n\t"
    "buffer_load_dwordx4 v57, s[64:67], s90 offen nt lds\n\t"
    "v_xor_b32 v58, 16, v57\n\t"
    "buffer_load_dwordx4 v58, s[64:67], s90 offen offset:1024 nt lds\n\t"
    "v_xor_b32 v58, 32, v57\n\t"
    "buffer_load_dwordx4 v58, s[64:67], s90 offen offset:2048 nt lds\n\t"
    "v_xor_b32 v58, 48, v57\n\t"
    "buffer_load_dwordx4 v58, s[64:67], s90 offen offset:3072 nt lds\n\t"
    "96:\n\t"
    "v_mfma_f32_16x16x32_bf16 v[0:3], v[96:99], v[16:19], v[0:3]\n\t"
    "v_mfma_f32_16x16x32_bf16 v[32:35], v[100:103], v[16:19], v[32:35]\n\t"
    "v_mfma_f32_16x16x32_bf16 v[36:39], v[104:107], v[16:19], v[36:39]\n\t"
    "v_mfma_f32_16x16x32_bf16 v[40:43], v[108:111], v[16:19], v[40:43]\n\t"
    "v_mfma_f32_16x16x32_bf16 v[44:47], v[112:115], v[16:19], v[44:47]\n\t"
    "v_mfma_f32_16x16x32_bf16 v[48:51], v[116:119], v[16:19], v[48:51]\n\t"
    "v_mfma_f32_16x16x32_bf16 v[52:55], v[120:123], v[16:19], v[52:55]\n\t"
    "v_mfma_f32_16x16x32_bf16 v[20:23], v[124:127], v[16:19], v[20:23]\n\t"
    "s_cbranch_vccnz 96f\n\t"
    "s_waitcnt vmcnt(4)\n\t"
    "96:\n\t"
    "s_waitcnt vmcnt(8)\n\t"
    TP(34)
    "ds_read_b64_tr_b16 v[128:129], v31 offset:4096\n\t"
    "v_xor_b32 v58, 32, v31\n\t"
    "ds_read_b64_tr_b16 v[132:133], v58 offset:4096\n\t"
    "v_xor_b32 v59, 64, v31\n\t"
    "ds_read_b64_tr_b16 v[136:137], v59 offset:4096\n\t"
    "v_xor_b32 v58, 96, v31\n\t"
    "ds_read_b64_tr_b16 v[140:141], v58 offset:4096\n\t"
    "v_xor_b32 v59, 128, v31\n\t"
    "ds_read_b64_tr_b16 v[144:145], v59 offset:4096\n\t"
    "v_xor_b32 v58, 160, v31\n\t"
    "ds_read_b64_tr_b16 v[148:149], v58 offset:4096\n\t"
    "v_xor_b32 v59, 192, v31\n\t"
    "ds_read_b64_tr_b16 v[152:153], v59 offset:4096\n\t"
    "v_xor_b32 v58, 224, v31\n\t"
    "ds_read_b64_tr_b16 v[156:157], v58 offset:4096\n\t"
    "s_waitcnt lgkmcnt(0)\n\t"
    "s_cbranch_vccz 96f\n\t"
    "s_add_u32 m0, %[ldsw], 4096\n\t"
    "s_add_u32 s90, s36, 16384\n\t"
    "buffer_load_dwordx4 v57, s[64:67], s90 offen nt lds\n\t"
    "v_xor_b32 v58, 16, v57\n\t"
    "buffer_load_dwordx4 v58, s[64:67], s90 offen offset:1024 nt lds\n\t"
    "v_xor_b32 v58, 32, v57\n\t"
    "buffer_load_dwordx4 v58, s[64:67], s90 offen offset:2048 nt lds\n\t"
    "v_xor_b32 v58, 48, v57\n\t"
    "buffer_load_dwordx4 v58, s[64:67], s90 offen offset:3072 nt lds\n\t"
    "96:\n\t"
    "s_cbranch_vccnz 96f\n\t"
    "s_waitcnt vmcnt(0)\n\t"
    "96:\n\t"
    "s_waitcnt vmcnt(8)\n\t"
    TP(35)
    "ds_read_b64_tr_b16 v[130:131], v31 offset:8192\n\t"
    "v_xor_b32 v58, 32, v31\n\t"
    "ds_read_b64_tr_b16 v[134:135], v58 offset:8192\n\t"
    "v_xor_b32 v59, 64, v31\n\t"
    "ds_read_b64_tr_b16 v[138:139], v59 offset:8192\n\t"
    "v_xor_b32 v58, 96, v31\n\t"
    "ds_read_b64_tr_b16 v[142:143], v58 offset:8192\n\t"
    "v_xor_b32 v59, 128, v31\n\t"
    "ds_read_b64_tr_b16 v[146:147], v59 offset:8192\n\t"
    "v_xor_b32 v58, 160, v31\n\t"
    "ds_read_b64_tr_b16 v[150:151], v58 offset:8192\n\t"
    "v_xor_b32 v59, 192, v31\n\t"
    "ds_read_b64_tr_b16 v[154:155], v59 offset:8192\n\t"
    "v_xor_b32 v58, 224, v31\n\t"
    "ds_read_b64_tr_b16 v[158:159], v58 offset:8192\n\t"
    "s_waitcnt lgkmcnt(7)\n\t"
    "v_mfma_f32_16x16x32_bf16 v[0:3], v[128:131], v[24:27], v[0:3]\n\t"
    "s_waitcnt lgkmcnt(6)\n\t"
    "v_mfma_f32_16x16x32_bf16 v[32:35], v[132:135], v[24:27], v[32:35]\n\t"
    "s_waitcnt lgkmcnt(5)\n\t"
    "v_mfma_f32_16x16x32_bf16 v[36:39], v[136:139], v[24:27], v[36:39]\n\t"
    "s_waitcnt lgkmcnt(4)\n\t"
    "v_mfma_f32_16x16x32_bf16 v[40:43], v[140:143], v[24:27], v[40:43]\n\t"
    "s_waitcnt lgkmcnt(3)\n\t"
    "v_mfma_f32_16x16x32_bf16 v[44:47], v[144:147], v[24:27], v[44:47]\n\t"
    "s_waitcnt lgkmcnt(2)\n\t"
    "v_mfma_f32_16x16x32_bf16 v[48:51], v[148:151], v[24:27], v[48:51]\n\t"
    "v_mov_b32_dpp v0, v32 row_shr:4 row_mask:0xf bank_mask:0x2\n\t"
    "v_mov_b32_dpp v1, v33 row_shr:4 row_mask:0xf bank_mask:0x2\n\t"
    "s_waitcnt lgkmcnt(1)\n\t"
    "v_mfma_f32_16x16x32_bf16 v[52:55], v[152:155], v[24:27], v[52:55]\n\t"
    "v_mov_b32_dpp v2, v34 row_shr:4 row_mask:0xf bank_mask:0x2\n\t"
    "v_mov_b32_dpp v3, v35 row_shr:4 row_mask:0xf bank_mask:0x2\n\t"
    "v_mov_b32_dpp v0, v36 row_shr:8 row_mask:0xf bank_mask:0x4\n\t"
    "s_waitcnt lgkmcnt(0)\n\t"
    "v_mfma_f32_16x16x32_bf16 v[20:23], v[156:159], v[24:27], v[20:23]\n\t"
    "v_mov_b32_dpp v1, v37 row_shr:8 row_mask:0xf bank_mask:0x4\n\t"
    "v_mov_b32_dpp v2, v38 row_shr:8 row_mask:0xf bank_mask:0x4\n\t"
    "v_mov_b32_dpp v3, v39 row_shr:8 row_mask:0xf bank_mask:0x4\n\t"
    "v_mov_b32_dpp v0, v40 row_shr:12 row_mask:0xf bank_mask:0x8\n\t"
    "v_mov_b32_dpp v1, v41 row_shr:12 row_mask:0xf bank_mask:0x8\n\t"
    "v_mov_b32_dpp v2, v42 row_shr:12 row_mask:0xf bank_mask:0x8\n\t"
    "v_mov_b32_dpp v3, v43 row_shr:12 row_mask:0xf bank_mask:0x8\n\t"
    "ds_write_b128 v4, v[0:3]\n\t"
    "v_mov_b32_dpp v44, v48 row_shr:4 row_mask:0xf bank_mask:0x2\n\t"
    "v_mov_b32_dpp v45, v49 row_shr:4 row_mask:0xf bank_mask:0x2\n\t"
    "v_mov_b32_dpp v46, v50 row_shr:4 row_mask:0xf bank_mask:0x2\n\t"
    "v_mov_b32_dpp v47, v51 row_shr:4 row_mask:0xf bank_mask:0x2\n\t"
    "v_mov_b32_dpp v44, v52 row_shr:8 row_mask:0xf bank_mask:0x4\n\t"
    "v_mov_b32_dpp v45, v53 row_shr:8 row_mask:0xf bank_mask:0x4\n\t"
    "v_mov_b32_dpp v46, v54 row_shr:8 row_mask:0xf bank_mask:0x4\n\t"
    "v_mov_b32_dpp v47, v55 row_shr:8 row_mask:0xf bank_mask:0x4\n\t"
    "v_mov_b32_dpp v44, v20 row_shr:12 row_mask:0xf bank_mask:0x8\n\t"
    "v_mov_b32_dpp v45, v21 row_shr:12 row_mask:0xf bank_mask:0x8\n\t"
    "v_mov_b32_dpp v46, v22 row_shr:12 row_mask:0xf bank_mask:0x8\n\t"
    "v_mov_b32_dpp v47, v23 row_shr:12 row_mask:0xf bank_mask:0x8\n\t"
    "ds_write_b128 v4, v[44:47] offset:256\n\t"
    "s_waitcnt lgkmcnt(0)\n\t"
    TP(14)
    "s_barrier\n\t"
    TP(15)
    "ds_read_b64 v[32:33], v28 offset:0\n\t"
    "ds_read_b64 v[34:35], v28 offset:2112\n\t"
    "ds_read_b64 v[36:37], v28 offset:4224\n\t"
    "ds_read_b64 v[38:39], v28 offset:6336\n\t"
    "s_waitcnt lgkmcnt(3)\n\t"
    "v_mul_f32_dpp v40, v60, v32 quad_perm:[0,0,0,0] row_mask:0xf bank_mask:0xf\n\t"
    "v_mul_f32_dpp v41, v60, v33 quad_perm:[0,0,0,0] row_mask:0xf bank_mask:0xf\n\t"
    "s_waitcnt lgkmcnt(2)\n\t"
    "v_fmac_f32_dpp v40, v60, v34 quad_perm:[1,1,1,1] row_mask:0xf bank_mask:0xf\n\t"
    "v_fmac_f32_dpp v41, v60, v35 quad_perm:[1,1,1,1] row_mask:0xf bank_mask:0xf\n\t"
    "s_waitcnt lgkmcnt(1)\n\t"
    "v_fmac_f32_dpp v40, v60, v36 quad_perm:[2,2,2,2] row_mask:0xf bank_mask:0xf\n\t"
    "v_fmac_f32_dpp v41, v60, v37 quad_perm:[2,2,2,2] row_mask:0xf bank_mask:0xf\n\t"
    "s_waitcnt lgkmcnt(0)\n\t"
    "v_fmac_f32_dpp v40, v60, v38 quad_perm:[3,3,3,3] row_mask:0xf bank_mask:0xf\n\t"
    "v_fmac_f32_dpp v41, v60, v39 quad_perm:[3,3,3,3] row_mask:0xf bank_mask:0xf\n\t"
    "v_cvt_pk_bf16_f32 v60, v40, v41\n\t"
    "v_min_u32 v60, -2, v60\n\t"
    "s_bitcmp1_b32 s25, 5\n\t"
    "s_cbranch_scc0 96f\n\t"
    "s_add_u32 m0, %[ldsw], 8192\n\t"
    "s_add_u32 s90, s36, 32768\n\t"
    "buffer_load_dwordx4 v57, s[64:67], s90 offen nt lds\n\t"
    "v_xor_b32 v58, 16, v57\n\t"
    "buffer_load_dwordx4 v58, s[64:67], s90 offen offset:1024 nt lds\n\t"
    "v_xor_b32 v58, 32, v57\n\t"
    "buffer_load_dwordx4 v58, s[64:67], s90 offen offset:2048 nt lds\n\t"
    "v_xor_b32 v58, 48, v57\n\t"
    "buffer_load_dwordx4 v58, s[64:67], s90 offen offset:3072 nt lds\n\t"
    "96:\n\t"
    "s_memrealtime s[86:87]\n\t"
    "v_lshlrev_b32 v0, 2, v29\n\t"
    "v_mov_b32 v8, -1\n\t"
    "v_mov_b32 v9, -1\n\t"
    "v_mov_b32 v10, -1\n\t"
    "v_mov_b32 v11, -1\n\t"
    "s_waitcnt lgkmcnt(0)\n\t"
    TP(16)
    STAMP2_STR
    "s_mov_b32 s88, s38\n\t"
    "67:\n\t"
    "s_sub_u32 s89, s86, s88\n\t"
    "s_cmp_le_i32 s89, " STR1(SLACK) "\n\t"
    "s_cbranch_scc1 66f\n\t"
    "s_add_u32 s88, s88, " STR1(GTICK) "\n\t"
    "s_bitset1_b32 s75, 31\n\t"
    "s_branch 67b\n\t"
    "66:\n\t"
    "s_memrealtime s[86:87]\n\t"
    "s_waitcnt lgkmcnt(0)\n\t"
    "s_sub_u32 s86, s86, s88\n\t"
    "s_cmp_lt_i32 s86, 0\n\t"
    "s_cbranch_scc1 66b\n\t"
    "buffer_store_dword v60, v29, %[rws], s31 offen sc1\n\t"
    "buffer_store_dword v61, v30, %[rws], s32 offen sc1\n\t"
    "s_memtime s[78:79]\n\t"
    "buffer_store_dword v12, v29, %[rws], s33 offen sc1\n\t"
    "buffer_store_dword v13, v30, %[rws], s34 offen sc1\n\t"
    "s_bitcmp1_b32 %[ldsw], 12\n\t"
    "s_cselect_b32 exec_hi, 0xffff, -1\n\t"
    "buffer_store_dwordx4 v[8:11], v0, %[rws], s37 offen sc1\n\t"
    "s_mov_b64 exec, -1\n\t"
    "s_nop 0\n\t"
    "s_branch 99f\n\t"
    "64:\n\t"
    "s_add_u32 s92, s92, 1\n\t"
    "s_cmp_gt_u32 s92, 16384\n\t"
    "s_cbranch_scc1 63b\n\t"
    "s_sleep 1\n\t"
    "s_branch 65f\n\t"
    "65:\n\t"
    "buffer_load_dwordx4 v[8:11], v51, %[rws], s30 offen sc1\n\t"
    "s_waitcnt vmcnt(0)\n\t"
    "s_branch 62b\n\t"
    "80:\n\t"
    "v_mbcnt_lo_u32_b32 v50, -1, 0\n\t"
    "v_mbcnt_hi_u32_b32 v50, -1, v50\n\t"
    "v_lshrrev_b32 v52, 4, v50\n\t"
    "v_lshlrev_b32 v47, 1, v52\n\t"
    "v_xor_b32 v47, v47, v50\n\t"
    "v_and_b32 v47, 15, v47\n\t"
    "v_lshlrev_b32 v47, 4, v47\n\t"
    "v_lshl_add_u32 v47, v52, 8, v47\n\t"
    "v_lshrrev_b32 v52, 2, v50\n\t"
    "v_lshlrev_b32 v31, 8, v52\n\t"
    "v_and_b32 v52, 7, v52\n\t"
    "v_lshl_add_u32 v31, v52, 5, v31\n\t"
    "v_and_b32 v52, 3, v50\n\t"
    "v_lshl_add_u32 v31, v52, 3, v31\n\t"
    "v_add_u32 v31, %[ldsw], v31\n\t"
    "v_and_b32 v51, 3, v50\n\t"
    "v_lshrrev_b32 v52, 4, v50\n\t"
    "v_lshl_add_u32 v51, v51, 2, v52\n\t"
    "v_mul_u32_u24 v51, 14592, v51\n\t"
    "v_and_b32 v52, 12, v50\n\t"
    "v_lshl_add_u32 v51, v52, 2, v51\n\t"
    "s_mov_b32 m0, s77\n\t"
    "s_lshl_b32 exec_lo, 0x11111111, %[w]\n\t"
    "s_mov_b32 exec_hi, exec_lo\n\t"
    "buffer_load_dwordx4 v51, %[rws], s30 offen sc1 lds\n\t"
    "s_mov_b64 exec, -1\n\t"
    "s_waitcnt vmcnt(9)\n\t"
    TP(30)
    "ds_read_b64_tr_b16 v[64:65], v31 offset:0\n\t"
    "v_xor_b32 v48, 32, v31\n\t"
    "ds_read_b64_tr_b16 v[68:69], v48 offset:0\n\t"
    "v_xor_b32 v49, 64, v31\n\t"
    "ds_read_b64_tr_b16 v[72:73], v49 offset:0\n\t"
    "v_xor_b32 v48, 96, v31\n\t"
    "ds_read_b64_tr_b16 v[76:77], v48 offset:0\n\t"
    "v_xor_b32 v49, 128, v31\n\t"
    "ds_read_b64_tr_b16 v[80:81], v49 offset:0\n\t"
    "v_xor_b32 v48, 160, v31\n\t"
    "ds_read_b64_tr_b16 v[84:85], v48 offset:0\n\t"
    "v_xor_b32 v49, 192, v31\n\t"
    "ds_read_b64_tr_b16 v[88:89], v49 offset:0\n\t"
    "v_xor_b32 v48, 224, v31\n\t"
    "ds_read_b64_tr_b16 v[92:93], v48 offset:0\n\t"
    "s_waitcnt lgkmcnt(0)\n\t"
    "s_add_u32 m0, %[ldsw], 0\n\t"
    "s_add_u32 s90, s35, 81920\n\t"
    "v_xor_b32 v48, 128, v47\n\t"
    "buffer_load_dwordx4 v47, s[56:59], s90 offen offset:0 nt lds\n\t"
    "buffer_load_dwordx4 v48, s[56:59], s90 offen offset:1024 nt lds\n\t"
    "buffer_load_dwordx4 v47, s[56:59], s90 offen offset:2048 nt lds\n\t"
    "buffer_load_dwordx4 v48, s[56:59], s90 offen offset:3072 nt lds\n\t"
    "v_and_b32 v5, 3, v50\n\t"
    "v_mul_u32_u24 v5, 528, v5\n\t"
    "v_add_u32 v5, s26, v5\n\t"
    "v_and_b32 v52, 12, v50\n\t"
    "v_lshl_add_u32 v4, v52, 4, v5\n\t"
    "v_and_b32 v52, 48, v50\n\t"
    "v_add_u32 v4, v4, v52\n\t"
    "v_lshl_add_u32 v28, v50, 3, s27\n\t"
    "v_lshlrev_b32 v30, 24, v50\n\t"
    "v_lshlrev_b32 v29, 2, v50\n\t"
    "s_mov_b32 s84, 0xf000f0\n\t"
    "s_mov_b32 s85, 0xf000f0\n\t"
    "s_mov_b32 s86, 0xf000f00\n\t"
    "s_mov_b32 s87, 0xf000f00\n\t"
    "s_mov_b32 s88, 0xf000f000\n\t"
    "s_mov_b32 s89, 0xf000f000\n\t"
    "v_mov_b32 v0, 0x3f803f80\n\t"
    "v_mov_b32 v1, 0x3f803f80\n\t"
    "v_mov_b32 v2, 0x3f803f80\n\t"
    "v_mov_b32 v3, 0x3f803f80\n\t"
    "s_mov_b32 s92, 0\n\t"
    "s_add_u32 s38, s38, s72\n\t"
    "s_add_u32 s90, s82, s39\n\t"
    "94:\n\t"
    "s_sub_u32 s91, s90, s38\n\t"
    "s_cmp_gt_i32 s91, 0\n\t"
    "s_cbranch_scc0 93f\n\t"
    "s_add_u32 s38, s38, " STR1(GTICK) "\n\t"
    "s_branch 94b\n\t"
    "93:\n\t"
    "s_waitcnt vmcnt(9)\n\t"
    TP(31)
    "ds_read_b64_tr_b16 v[66:67], v31 offset:4096\n\t"
    "v_xor_b32 v48, 32, v31\n\t"
    "ds_read_b64_tr_b16 v[70:71], v48 offset:4096\n\t"
    "v_xor_b32 v49, 64, v31\n\t"
    "ds_read_b64_tr_b16 v[74:75], v49 offset:4096\n\t"
    "v_xor_b32 v48, 96, v31\n\t"
    "ds_read_b64_tr_b16 v[78:79], v48 offset:4096\n\t"
    "v_xor_b32 v49, 128, v31\n\t"
    "ds_read_b64_tr_b16 v[82:83], v49 offset:4096\n\t"
    "v_xor_b32 v48, 160, v31\n\t"
    "ds_read_b64_tr_b16 v[86:87], v48 offset:4096\n\t"
    "v_xor_b32 v49, 192, v31\n\t"
    "ds_read_b64_tr_b16 v[90:91], v49 offset:4096\n\t"
    "v_xor_b32 v48, 224, v31\n\t"
    "ds_read_b64_tr_b16 v[94:95], v48 offset:4096\n\t"
    "s_waitcnt lgkmcnt(0)\n\t"
    "s_add_u32 m0, %[ldsw], 4096\n\t"
    "s_add_u32 s90, s35, 98304\n\t"
    "v_xor_b32 v48, 128, v47\n\t"
    "buffer_load_dwordx4 v47, s[56:59], s90 offen offset:0 nt lds\n\t"
    "buffer_load_dwordx4 v48, s[56:59], s90 offen offset:1024 nt lds\n\t"
    "buffer_load_dwordx4 v47, s[56:59], s90 offen offset:2048 nt lds\n\t"
    "buffer_load_dwordx4 v48, s[56:59], s90 offen offset:3072 nt lds\n\t"
    "s_waitcnt vmcnt(8)\n\t"
    TP(32)
    "ds_read_b64_tr_b16 v[96:97], v31 offset:8192\n\t"
    "v_xor_b32 v48, 32, v31\n\t"
    "ds_read_b64_tr_b16 v[100:101], v48 offset:8192\n\t"
    "v_xor_b32 v49, 64, v31\n\t"
    "ds_read_b64_tr_b16 v[104:105], v49 offset:8192\n\t"
    "v_xor_b32 v48, 96, v31\n\t"
    "ds_read_b64_tr_b16 v[108:109], v48 offset:8192\n\t"
    "v_xor_b32 v49, 128, v31\n\t"
    "ds_read_b64_tr_b16 v[112:113], v49 offset:8192\n\t"
    "v_xor_b32 v48, 160, v31\n\t"
    "ds_read_b64_tr_b16 v[116:117], v48 offset:8192\n\t"
    "v_xor_b32 v49, 192, v31\n\t"
    "ds_read_b64_tr_b16 v[120:121], v49 offset:8192\n\t"
    "v_xor_b32 v48, 224, v31\n\t"
    "ds_read_b64_tr_b16 v[124:125], v48 offset:8192\n\t"
    "s_waitcnt lgkmcnt(0)\n\t"
    "s_add_u32 m0, %[ldsw], 8192\n\t"
    "s_add_u32 s90, s35, 114688\n\t"
    "v_xor_b32 v48, 128, v47\n\t"
    "buffer_load_dwordx4 v47, s[56:59], s90 offen offset:0 nt lds\n\t"
    "buffer_load_dwordx4 v48, s[56:59], s90 offen offset:1024 nt lds\n\t"
    "buffer_load_dwordx4 v47, s[56:59], s90 offen offset:2048 nt lds\n\t"
    "buffer_load_dwordx4 v48, s[56:59], s90 offen offset:3072 nt lds\n\t"
    "v_lshl_add_u32 v52, v50, 4, s77\n\t"
    "s_barrier\n\t"
    "ds_read_b128 v[8:11], v52\n\t"
    "s_waitcnt lgkmcnt(0)\n\t"
    "82:\n\t"
    "v_max3_u32 v52, v8, v9, v10\n\t"
    "v_max_u32 v52, v52, v11\n\t"
    "v_cmp_eq_u32 vcc, -1, v52\n\t"
    "s_cbranch_vccnz 84f\n\t"
    "83:\n\t"
    "v_mov_b32_dpp v12, v8 row_ror:12 row_mask:0xf bank_mask:0xf\n\t"
    "v_mov_b32_dpp v13, v9 row_ror:12 row_mask:0xf bank_mask:0xf\n\t"
    "v_mov_b32_dpp v14, v10 row_ror:12 row_mask:0xf bank_mask:0xf\n\t"
    "v_mov_b32_dpp v15, v11 row_ror:12 row_mask:0xf bank_mask:0xf\n\t"
    "v_mov_b32_dpp v16, v8 row_ror:8 row_mask:0xf bank_mask:0xf\n\t"
    "v_mov_b32_dpp v17, v9 row_ror:8 row_mask:0xf bank_mask:0xf\n\t"
    "v_mov_b32_dpp v18, v10 row_ror:8 row_mask:0xf bank_mask:0xf\n\t"
    "v_mov_b32_dpp v19, v11 row_ror:8 row_mask:0xf bank_mask:0xf\n\t"
    "v_mov_b32_dpp v20, v8 row_ror:4 row_mask:0xf bank_mask:0xf\n\t"
    "v_mov_b32_dpp v21, v9 row_ror:4 row_mask:0xf bank_mask:0xf\n\t"
    "v_mov_b32_dpp v22, v10 row_ror:4 row_mask:0xf bank_mask:0xf\n\t"
    "v_mov_b32_dpp v23, v11 row_ror:4 row_mask:0xf bank_mask:0xf\n\t"
    "s_nop 1\n\t"
    "v_mfma_f32_16x16x32_bf16 v[56:59], a[0:3], v[8:11], 0\n\t"
    "v_mfma_f32_16x16x32_bf16 v[56:59], a[4:7], v[12:15], v[56:59]\n\t"
    "v_mfma_f32_16x16x32_bf16 v[56:59], a[8:11], v[16:19], v[56:59]\n\t"
    "v_mfma_f32_16x16x32_bf16 v[56:59], a[12:15], v[20:23], v[56:59]\n\t"
    "v_mfma_f32_16x16x32_bf16 v[60:63], a[16:19], v[8:11], 0\n\t"
    "v_mfma_f32_16x16x32_bf16 v[60:63], a[20:23], v[12:15], v[60:63]\n\t"
    "v_mfma_f32_16x16x32_bf16 v[60:63], a[24:27], v[16:19], v[60:63]\n\t"
    "v_mfma_f32_16x16x32_bf16 v[60:63], a[28:31], v[20:23], v[60:63]\n\t"
    "v_mfma_f32_16x16x32_bf16 v[32:35], a[36:39], v[8:11], 0\n\t"
    "v_mfma_f32_16x16x32_bf16 v[32:35], a[40:43], v[12:15], v[32:35]\n\t"
    "v_mfma_f32_16x16x32_bf16 v[32:35], a[44:47], v[16:19], v[32:35]\n\t"
    "v_mfma_f32_16x16x32_bf16 v[32:35], a[32:35], v[20:23], v[32:35]\n\t"
    "v_mfma_f32_16x16x32_bf16 v[36:39], a[52:55], v[8:11], 0\n\t"
    "s_nop 0\n\t"
    "v_mfma_f32_16x16x32_bf16 v[36:39], a[56:59], v[12:15], v[36:39]\n\t"
    "s_nop 0\n\t"
    "v_mfma_f32_16x16x32_bf16 v[36:39], a[60:63], v[16:19], v[36:39]\n\t"
    "s_nop 0\n\t"
    "v_mfma_f32_16x16x32_bf16 v[36:39], a[48:51], v[20:23], v[36:39]\n\t"
    "s_nop 0\n\t"
    "v_mfma_f32_16x16x32_bf16 v[40:43], a[72:75], v[8:11], 0\n\t"
    "v_cndmask_b32_e64 v56, v56, v32, s[84:85]\n\t"
    "v_mfma_f32_16x16x32_bf16 v[40:43], a[76:79], v[12:15], v[40:43]\n\t"
    "v_cndmask_b32_e64 v57, v57, v33, s[84:85]\n\t"
    "v_mfma_f32_16x16x32_bf16 v[40:43], a[64:67], v[16:19], v[40:43]\n\t"
    "v_cndmask_b32_e64 v58, v58, v34, s[84:85]\n\t"
    "v_mfma_f32_16x16x32_bf16 v[40:43], a[68:71], v[20:23], v[40:43]\n\t"
    "v_cndmask_b32_e64 v59, v59, v35, s[84:85]\n\t"
    "v_mfma_f32_16x16x32_bf16 v[32:35], a[88:91], v[8:11], 0\n\t"
    "v_cndmask_b32_e64 v60, v60, v36, s[84:85]\n\t"
    "v_mfma_f32_16x16x32_bf16 v[32:35], a[92:95], v[12:15], v[32:35]\n\t"
    "v_cndmask_b32_e64 v61, v61, v37, s[84:85]\n\t"
    "v_mfma_f32_16x16x32_bf16 v[32:35], a[80:83], v[16:19], v[32:35]\n\t"
    "v_cndmask_b32_e64 v62, v62, v38, s[84:85]\n\t"
    "v_mfma_f32_16x16x32_bf16 v[32:35], a[84:87], v[20:23], v[32:35]\n\t"
    "v_cndmask_b32_e64 v63, v63, v39, s[84:85]\n\t"
    "v_mfma_f32_16x16x32_bf16 v[36:39], a[108:111], v[8:11], 0\n\t"
    "v_cndmask_b32_e64 v56, v56, v40, s[86:87]\n\t"
    "v_mfma_f32_16x16x32_bf16 v[36:39], a[96:99], v[12:15], v[36:39]\n\t"
    "v_cndmask_b32_e64 v57, v57, v41, s[86:87]\n\t"
    "v_mfma_f32_16x16x32_bf16 v[36:39], a[100:103], v[16:19], v[36:39]\n\t"
    "v_cndmask_b32_e64 v58, v58, v42, s[86:87]\n\t"
    "v_mfma_f32_16x16x32_bf16 v[36:39], a[104:107], v[20:23], v[36:39]\n\t"
    "v_cndmask_b32_e64 v59, v59, v43, s[86:87]\n\t"
    "v_mfma_f32_16x16x32_bf16 v[40:43], a[124:127], v[8:11], 0\n\t"
    "v_cndmask_b32_e64 v60, v60, v32, s[86:87]\n\t"
    "v_mfma_f32_16x16x32_bf16 v[40:43], a[112:115], v[12:15], v[40:43]\n\t"
    "v_cndmask_b32_e64 v61, v61, v33, s[86:87]\n\t"
    "v_mfma_f32_16x16x32_bf16 v[40:43], a[116:119], v[16:19], v[40:43]\n\t"
    "v_cndmask_b32_e64 v62, v62, v34, s[86:87]\n\t"
    "v_mfma_f32_16x16x32_bf16 v[40:43], a[120:123], v[20:23], v[40:43]\n\t"
    "v_cndmask_b32_e64 v63, v63, v35, s[86:87]\n\t"
    "v_cndmask_b32_e64 v56, v56, v36, s[88:89]\n\t"
    "v_cndmask_b32_e64 v57, v57, v37, s[88:89]\n\t"
    "v_cndmask_b32_e64 v58, v58, v38, s[88:89]\n\t"
    "v_cndmask_b32_e64 v59, v59, v39, s[88:89]\n\t"
    "v_max3_f32 v6, v56, v57, v58\n\t"
    "v_max_f32 v6, v6, v59\n\t"
    "s_nop 1\n\t"
    "v_cndmask_b32_e64 v60, v60, v40, s[88:89]\n\t"
    "v_cndmask_b32_e64 v61, v61, v41, s[88:89]\n\t"
    "v_cndmask_b32_e64 v62, v62, v42, s[88:89]\n\t"
    "v_cndmask_b32_e64 v63, v63, v43, s[88:89]\n\t"
    "v_max3_f32 v6, v6, v60, v61\n\t"
    "v_max3_f32 v6, v6, v62, v63\n\t"
    "s_nop 1\n\t"
    "v_max_f32_dpp v6, v6, v6 row_ror:4 row_mask:0xf bank_mask:0xf\n\t"
    "s_nop 1\n\t"
    "v_max_f32_dpp v6, v6, v6 row_ror:8 row_mask:0xf bank_mask:0xf\n\t"
    "v_mov_b32 v46, v6\n\t"
    "s_nop 1\n\t"
    "v_permlane16_swap_b32 v6, v46\n\t"
    "v_max_f32 v6, v6, v46\n\t"
    "v_mov_b32 v46, v6\n\t"
    "s_nop 1\n\t"
    "v_permlane32_swap_b32 v6, v46\n\t"
    "v_max_f32 v6, v6, v46\n\t"
    "v_mul_f32 v6, s28, v6\n\t"
    "v_fma_f32 v56, v56, s28, -v6\n\t"
    "v_fma_f32 v57, v57, s28, -v6\n\t"
    "v_fma_f32 v58, v58, s28, -v6\n\t"
    "v_fma_f32 v59, v59, s28, -v6\n\t"
    "v_fma_f32 v60, v60, s28, -v6\n\t"
    "v_fma_f32 v61, v61, s28, -v6\n\t"
    "v_fma_f32 v62, v62, s28, -v6\n\t"
    "v_fma_f32 v63, v63, s28, -v6\n\t"
    "v_exp_f32 v56, v56\n\t"
    "v_exp_f32 v57, v57\n\t"
    "v_exp_f32 v58, v58\n\t"
    "v_exp_f32 v59, v59\n\t"
    "v_exp_f32 v60, v60\n\t"
    "v_exp_f32 v61, v61\n\t"
    "v_exp_f32 v62, v62\n\t"
    "v_exp_f32 v63, v63\n\t"
    "v_cvt_pk_bf16_f32 v8, v56, v57\n\t"
    "v_cvt_pk_bf16_f32 v9, v58, v59\n\t"
    "v_cvt_pk_bf16_f32 v10, v60, v61\n\t"
    "v_cvt_pk_bf16_f32 v11, v62, v63\n\t"
    "s_nop 1\n\t"
    "v_mfma_f32_16x16x32_bf16 v[56:59], v[0:3], v[8:11], 0\n\t"
    "v_mov_b32_dpp v12, v8 row_ror:12 row_mask:0xf bank_mask:0xf\n\t"
    "v_mov_b32_dpp v13, v9 row_ror:12 row_mask:0xf bank_mask:0xf\n\t"
    "v_mov_b32_dpp v14, v10 row_ror:12 row_mask:0xf bank_mask:0xf\n\t"
    "v_mov_b32_dpp v15, v11 row_ror:12 row_mask:0xf bank_mask:0xf\n\t"
    "v_mov_b32_dpp v16, v8 row_ror:8 row_mask:0xf bank_mask:0xf\n\t"
    "v_mov_b32_dpp v17, v9 row_ror:8 row_mask:0xf bank_mask:0xf\n\t"
    "v_mov_b32_dpp v18, v10 row_ror:8 row_mask:0xf bank_mask:0xf\n\t"
    "v_mov_b32_dpp v19, v11 row_ror:8 row_mask:0xf bank_mask:0xf\n\t"
    "v_add_f32_dpp v7, v56, v56 row_ror:4 row_mask:0xf bank_mask:0xf\n\t"
    "v_mov_b32 v57, v47\n\t"
    "s_nop 0\n\t"
    "v_add_f32_dpp v7, v7, v7 row_ror:8 row_mask:0xf bank_mask:0xf\n\t"
    "ds_write_b64 v5, v[6:7] offset:512\n\t"
    "ds_read_b64_tr_b16 v[32:33], v31 offset:20480\n\t"
    "ds_read_b64_tr_b16 v[34:35], v31 offset:24576\n\t"
    "v_xor_b32 v59, 32, v31\n\t"
    "ds_read_b64_tr_b16 v[36:37], v59 offset:20480\n\t"
    "ds_read_b64_tr_b16 v[38:39], v59 offset:24576\n\t"
    "v_xor_b32 v58, 64, v31\n\t"
    "ds_read_b64_tr_b16 v[40:41], v58 offset:20480\n\t"
    "ds_read_b64_tr_b16 v[42:43], v58 offset:24576\n\t"
    "v_xor_b32 v59, 96, v31\n\t"
    "ds_read_b64_tr_b16 v[44:45], v59 offset:20480\n\t"
    "ds_read_b64_tr_b16 v[46:47], v59 offset:24576\n\t"
    "v_xor_b32 v58, 128, v31\n\t"
    "ds_read_b64_tr_b16 v[48:49], v58 offset:20480\n\t"
    "ds_read_b64_tr_b16 v[50:51], v58 offset:24576\n\t"
    "v_xor_b32 v59, 160, v31\n\t"
    "ds_read_b64_tr_b16 v[52:53], v59 offset:20480\n\t"
    "ds_read_b64_tr_b16 v[54:55], v59 offset:24576\n\t"
    "v_xor_b32 v58, 192, v31\n\t"
    "ds_read_b64_tr_b16 v[20:21], v58 offset:20480\n\t"
    "ds_read_b64_tr_b16 v[22:23], v58 offset:24576\n\t"
    "v_xor_b32 v59, 224, v31\n\t"
    "ds_read_b64_tr_b16 v[24:25], v59 offset:20480\n\t"
    "ds_read_b64_tr_b16 v[26:27], v59 offset:24576\n\t"
    "s_waitcnt lgkmcnt(0)\n\t"
    "v_mfma_f32_16x16x32_bf16 v[0:3], v[32:35], v[8:11], 0\n\t"
    "v_mfma_f32_16x16x32_bf16 v[32:35], v[36:39], v[8:11], 0\n\t"
    "v_mfma_f32_16x16x32_bf16 v[36:39], v[40:43], v[8:11], 0\n\t"
    "v_mfma_f32_16x16x32_bf16 v[40:43], v[44:47], v[8:11], 0\n\t"
    "v_mfma_f32_16x16x32_bf16 v[44:47], v[48:51], v[8:11], 0\n\t"
    "v_mfma_f32_16x16x32_bf16 v[48:51], v[52:55], v[8:11], 0\n\t"
    "v_mfma_f32_16x16x32_bf16 v[52:55], v[20:23], v[8:11], 0\n\t"
    "v_mfma_f32_16x16x32_bf16 v[20:23], v[24:27], v[8:11], 0\n\t"
    "v_mfma_f32_16x16x32_bf16 v[0:3], v[64:67], v[12:15], v[0:3]\n\t"
    "v_mov_b32_dpp v24, v8 row_ror:4 row_mask:0xf bank_mask:0xf\n\t"
    "v_mfma_f32_16x16x32_bf16 v[32:35], v[68:71], v[12:15], v[32:35]\n\t"
    "v_mov_b32_dpp v25, v9 row_ror:4 row_mask:0xf bank_mask:0xf\n\t"
    "v_mfma_f32_16x16x32_bf16 v[36:39], v[72:75], v[12:15], v[36:39]\n\t"
    "v_mov_b32_dpp v26, v10 row_ror:4 row_mask:0xf bank_mask:0xf\n\t"
    "v_mfma_f32_16x16x32_bf16 v[40:43], v[76:79], v[12:15], v[40:43]\n\t"
    "v_mov_b32_dpp v27, v11 row_ror:4 row_mask:0xf bank_mask:0xf\n\t"
    "v_mfma_f32_16x16x32_bf16 v[44:47], v[80:83], v[12:15], v[44:47]\n\t"
    "v_and_b32 v62, 12, v29\n\t"
    "v_mfma_f32_16x16x32_bf16 v[48:51], v[84:87], v[12:15], v[48:51]\n\t"
    "v_mul_u32_u24 v62, 528, v62\n\t"
    "v_mfma_f32_16x16x32_bf16 v[52:55], v[88:91], v[12:15], v[52:55]\n\t"
    "v_add_u32 v62, s27, v62\n\t"
    "v_mfma_f32_16x16x32_bf16 v[20:23], v[92:95], v[12:15], v[20:23]\n\t"
    "ds_read_b64 v[12:13], v28\n\t"
    "s_waitcnt lgkmcnt(0)\n\t"
    "s_barrier\n\t"
    "ds_read_b64 v[60:61], v62 offset:512\n\t"
    "s_waitcnt lgkmcnt(0)\n\t"
    "s_nop 1\n\t"
    "v_max_f32_dpp v62, v60, v60 quad_perm:[1,0,3,2] row_mask:0xf bank_mask:0xf\n\t"
    "s_nop 1\n\t"
    "v_max_f32_dpp v62, v62, v62 quad_perm:[2,3,0,1] row_mask:0xf bank_mask:0xf\n\t"
    "v_sub_f32 v60, v60, v62\n\t"
    "v_exp_f32 v60, v60\n\t"
    "s_nop 1\n\t"
    "v_mul_f32 v61, v60, v61\n\t"
    "s_nop 1\n\t"
    "v_add_f32_dpp v63, v61, v61 quad_perm:[1,0,3,2] row_mask:0xf bank_mask:0xf\n\t"
    "s_nop 1\n\t"
    "v_add_f32_dpp v63, v63, v63 quad_perm:[2,3,0,1] row_mask:0xf bank_mask:0xf\n\t"
    "v_log_f32 v61, v63\n\t"
    "v_rcp_f32 v63, v63\n\t"
    "s_nop 1\n\t"
    "v_add_f32 v61, v61, v62\n\t"
    "v_mul_f32 v60, v60, v63\n\t"
    "v_min_u32 v61, -2, v61\n\t"
    "s_bitcmp1_b32 s25, 4\n\t"
    "s_cselect_b64 vcc, -1, 0\n\t"
    "s_waitcnt vmcnt(8)\n\t"
    TP(33)
    "ds_read_b64_tr_b16 v[98:99], v31 offset:0\n\t"
    "v_xor_b32 v58, 32, v31\n\t"
    "ds_read_b64_tr_b16 v[102:103], v58 offset:0\n\t"
    "v_xor_b32 v59, 64, v31\n\t"
    "ds_read_b64_tr_b16 v[106:107], v59 offset:0\n\t"
    "v_xor_b32 v58, 96, v31\n\t"
    "ds_read_b64_tr_b16 v[110:111], v58 offset:0\n\t"
    "v_xor_b32 v59, 128, v31\n\t"
    "ds_read_b64_tr_b16 v[114:115], v59 offset:0\n\t"
    "v_xor_b32 v58, 160, v31\n\t"
    "ds_read_b64_tr_b16 v[118:119], v58 offset:0\n\t"
    "v_xor_b32 v59, 192, v31\n\t"
    "ds_read_b64_tr_b16 v[122:123], v59 offset:0\n\t"
    "v_xor_b32 v58, 224, v31\n\t"
    "ds_read_b64_tr_b16 v[126:127], v58 offset:0\n\t"
    "s_waitcnt lgkmcnt(0)\n\t"
    "v_mbcnt_lo_u32_b32 v59, -1, 0\n\t"
    "v_mbcnt_hi_u32_b32 v59, -1, v59\n\t"
    "v_lshrrev_b32 v58, 2, v59\n\t"
    "v_and_b32 v59, 3, v59\n\t"
    "v_lshlrev_b32 v59, 4, v59\n\t"
    "v_lshl_add_u32 v58, v58, 10, v59\n\t"
    "s_add_u32 m0, %[ldsw], 12288\n\t"
    "s_add_u32 s90, %[wo_off], 0\n\t"
    "buffer_load_dwordx4 v58, s[60:63], s90 offen lds\n\t"
    "s_add_u32 m0, %[ldsw], 13312\n\t"
    "s_add_u32 s90, %[wo_off], 16384\n\t"
    "buffer_load_dwordx4 v58, s[60:63], s90 offen lds\n\t"
    "s_add_u32 m0, %[ldsw], 14336\n\t"
    "s_add_u32 s90, %[wo_off], 32768\n\t"
    "buffer_load_dwordx4 v58, s[60:63], s90 offen lds\n\t"
    "s_add_u32 m0, %[ldsw], 15360\n\t"
    "s_add_u32 s90, %[wo_off], 49152\n\t"
    "buffer_load_dwordx4 v58, s[60:63], s90 offen lds\n\t"
    "v_mfma_f32_16x16x32_bf16 v[0:3], v[96:99], v[16:19], v[0:3]\n\t"
    "v_mfma_f32_16x16x32_bf16 v[32:35], v[100:103], v[16:19], v[32:35]\n\t"
    "v_mfma_f32_16x16x32_bf16 v[36:39], v[104:107], v[16:19], v[36:39]\n\t"
    "v_mfma_f32_16x16x32_bf16 v[40:43], v[108:111], v[16:19], v[40:43]\n\t"
    "v_mfma_f32_16x16x32_bf16 v[44:47], v[112:115], v[16:19], v[44:47]\n\t"
    "v_mfma_f32_16x16x32_bf16 v[48:51], v[116:119], v[16:19], v[48:51]\n\t"
    "v_mfma_f32_16x16x32_bf16 v[52:55], v[120:123], v[16:19], v[52:55]\n\t"
    "v_mfma_f32_16x16x32_bf16 v[20:23], v[124:127], v[16:19], v[20:23]\n\t"
    "s_waitcnt vmcnt(8)\n\t"
    TP(34)
    "ds_read_b64_tr_b16 v[128:129], v31 offset:4096\n\t"
    "v_xor_b32 v58, 32, v31\n\t"
    "ds_read_b64_tr_b16 v[132:133], v58 offset:4096\n\t"
    "v_xor_b32 v59, 64, v31\n\t"
    "ds_read_b64_tr_b16 v[136:137], v59 offset:4096\n\t"
    "v_xor_b32 v58, 96, v31\n\t"
    "ds_read_b64_tr_b16 v[140:141], v58 offset:4096\n\t"
    "v_xor_b32 v59, 128, v31\n\t"
    "ds_read_b64_tr_b16 v[144:145], v59 offset:4096\n\t"
    "v_xor_b32 v58, 160, v31\n\t"
    "ds_read_b64_tr_b16 v[148:149], v58 offset:4096\n\t"
    "v_xor_b32 v59, 192, v31\n\t"
    "ds_read_b64_tr_b16 v[152:153], v59 offset:4096\n\t"
    "v_xor_b32 v58, 224, v31\n\t"
    "ds_read_b64_tr_b16 v[156:157], v58 offset:4096\n\t"
    "s_waitcnt lgkmcnt(0)\n\t"
    "v_mbcnt_lo_u32_b32 v59, -1, 0\n\t"
    "v_mbcnt_hi_u32_b32 v59, -1, v59\n\t"
    "v_lshrrev_b32 v58, 2, v59\n\t"
    "v_and_b32 v59, 3, v59\n\t"
    "v_lshlrev_b32 v59, 4, v59\n\t"
    "v_lshl_add_u32 v58, v58, 10, v59\n\t"
    "s_add_u32 m0, %[ldsw], 16384\n\t"
    "s_add_u32 s90, %[wo_off], 65536\n\t"
    "buffer_load_dwordx4 v58, s[60:63], s90 offen lds\n\t"
    "s_add_u32 m0, %[ldsw], 17408\n\t"
    "s_add_u32 s90, %[wo_off], 81920\n\t"
    "buffer_load_dwordx4 v58, s[60:63], s90 offen lds\n\t"
    "s_add_u32 m0, %[ldsw], 18432\n\t"
    "s_add_u32 s90, %[wo_off], 98304\n\t"
    "buffer_load_dwordx4 v58, s[60:63], s90 offen lds\n\t"
    "s_add_u32 m0, %[ldsw], 19456\n\t"
    "s_add_u32 s90, %[wo_off], 114688\n\t"
    "buffer_load_dwordx4 v58, s[60:63], s90 offen lds\n\t"
    "s_waitcnt vmcnt(8)\n\t"
    TP(35)
    "ds_read_b64_tr_b16 v[130:131], v31 offset:8192\n\t"
    "v_xor_b32 v58, 32, v31\n\t"
    "ds_read_b64_tr_b16 v[134:135], v58 offset:8192\n\t"
    "v_xor_b32 v59, 64, v31\n\t"
    "ds_read_b64_tr_b16 v[138:139], v59 offset:8192\n\t"
    "v_xor_b32 v58, 96, v31\n\t"
    "ds_read_b64_tr_b16 v[142:143], v58 offset:8192\n\t"
    "v_xor_b32 v59, 128, v31\n\t"
    "ds_read_b64_tr_b16 v[146:147], v59 offset:8192\n\t"
    "v_xor_b32 v58, 160, v31\n\t"
    "ds_read_b64_tr_b16 v[150:151], v58 offset:8192\n\t"
    "v_xor_b32 v59, 192, v31\n\t"
    "ds_read_b64_tr_b16 v[154:155], v59 offset:8192\n\t"
    "v_xor_b32 v58, 224, v31\n\t"
    "ds_read_b64_tr_b16 v[158:159], v58 offset:8192\n\t"
    "s_waitcnt lgkmcnt(7)\n\t"
    "v_mfma_f32_16x16x32_bf16 v[0:3], v[128:131], v[24:27], v[0:3]\n\t"
    "s_waitcnt lgkmcnt(6)\n\t"
    "v_mfma_f32_16x16x32_bf16 v[32:35], v[132:135], v[24:27], v[32:35]\n\t"
    "s_waitcnt lgkmcnt(5)\n\t"
    "v_mfma_f32_16x16x32_bf16 v[36:39], v[136:139], v[24:27], v[36:39]\n\t"
    "s_waitcnt lgkmcnt(4)\n\t"
    "v_mfma_f32_16x16x32_bf16 v[40:43], v[140:143], v[24:27], v[40:43]\n\t"
    "s_waitcnt lgkmcnt(3)\n\t"
    "v_mfma_f32_16x16x32_bf16 v[44:47], v[144:147], v[24:27], v[44:47]\n\t"
    "s_waitcnt lgkmcnt(2)\n\t"
    "v_mfma_f32_16x16x32_bf16 v[48:51], v[148:151], v[24:27], v[48:51]\n\t"
    "v_mov_b32_dpp v0, v32 row_shr:4 row_mask:0xf bank_mask:0x2\n\t"
    "v_mov_b32_dpp v1, v33 row_shr:4 row_mask:0xf bank_mask:0x2\n\t"
    "s_waitcnt lgkmcnt(1)\n\t"
    "v_mfma_f32_16x16x32_bf16 v[52:55], v[152:155], v[24:27], v[52:55]\n\t"
    "v_mov_b32_dpp v2, v34 row_shr:4 row_mask:0xf bank_mask:0x2\n\t"
    "v_mov_b32_dpp v3, v35 row_shr:4 row_mask:0xf bank_mask:0x2\n\t"
    "v_mov_b32_dpp v0, v36 row_shr:8 row_mask:0xf bank_mask:0x4\n\t"
    "s_waitcnt lgkmcnt(0)\n\t"
    "v_mfma_f32_16x16x32_bf16 v[20:23], v[156:159], v[24:27], v[20:23]\n\t"
    "v_mov_b32_dpp v1, v37 row_shr:8 row_mask:0xf bank_mask:0x4\n\t"
    "v_mov_b32_dpp v2, v38 row_shr:8 row_mask:0xf bank_mask:0x4\n\t"
    "v_mov_b32_dpp v3, v39 row_shr:8 row_mask:0xf bank_mask:0x4\n\t"
    "v_mov_b32_dpp v0, v40 row_shr:12 row_mask:0xf bank_mask:0x8\n\t"
    "v_mov_b32_dpp v1, v41 row_shr:12 row_mask:0xf bank_mask:0x8\n\t"
    "v_mov_b32_dpp v2, v42 row_shr:12 row_mask:0xf bank_mask:0x8\n\t"
    "v_mov_b32_dpp v3, v43 row_shr:12 row_mask:0xf bank_mask:0x8\n\t"
    "ds_write_b128 v4, v[0:3]\n\t"
    "v_mov_b32_dpp v44, v48 row_shr:4 row_mask:0xf bank_mask:0x2\n\t"
    "v_mov_b32_dpp v45, v49 row_shr:4 row_mask:0xf bank_mask:0x2\n\t"
    "v_mov_b32_dpp v46, v50 row_shr:4 row_mask:0xf bank_mask:0x2\n\t"
    "v_mov_b32_dpp v47, v51 row_shr:4 row_mask:0xf bank_mask:0x2\n\t"
    "v_mov_b32_dpp v44, v52 row_shr:8 row_mask:0xf bank_mask:0x4\n\t"
    "v_mov_b32_dpp v45, v53 row_shr:8 row_mask:0xf bank_mask:0x4\n\t"
    "v_mov_b32_dpp v46, v54 row_shr:8 row_mask:0xf bank_mask:0x4\n\t"
    "v_mov_b32_dpp v47, v55 row_shr:8 row_mask:0xf bank_mask:0x4\n\t"
    "v_mov_b32_dpp v44, v20 row_shr:12 row_mask:0xf bank_mask:0x8\n\t"
    "v_mov_b32_dpp v45, v21 row_shr:12 row_mask:0xf bank_mask:0x8\n\t"
    "v_mov_b32_dpp v46, v22 row_shr:12 row_mask:0xf bank_mask:0x8\n\t"
    "v_mov_b32_dpp v47, v23 row_shr:12 row_mask:0xf bank_mask:0x8\n\t"
    "ds_write_b128 v4, v[44:47] offset:256\n\t"
    "s_waitcnt lgkmcnt(0)\n\t"
    TP(14)
    "s_barrier\n\t"
    TP(15)
    "ds_read_b64 v[32:33], v28 offset:0\n\t"
    "ds_read_b64 v[34:35], v28 offset:2112\n\t"
    "ds_read_b64 v[36:37], v28 offset:4224\n\t"
    "ds_read_b64 v[38:39], v28 offset:6336\n\t"
    "s_waitcnt lgkmcnt(3)\n\t"
    "v_mul_f32_dpp v40, v60, v32 quad_perm:[0,0,0,0] row_mask:0xf bank_mask:0xf\n\t"
    "v_mul_f32_dpp v41, v60, v33 quad_perm:[0,0,0,0] row_mask:0xf bank_mask:0xf\n\t"
    "s_waitcnt lgkmcnt(2)\n\t"
    "v_fmac_f32_dpp v40, v60, v34 quad_perm:[1,1,1,1] row_mask:0xf bank_mask:0xf\n\t"
    "v_fmac_f32_dpp v41, v60, v35 quad_perm:[1,1,1,1] row_mask:0xf bank_mask:0xf\n\t"
    "s_waitcnt lgkmcnt(1)\n\t"
    "v_fmac_f32_dpp v40, v60, v36 quad_perm:[2,2,2,2] row_mask:0xf bank_mask:0xf\n\t"
    "v_fmac_f32_dpp v41, v60, v37 quad_perm:[2,2,2,2] row_mask:0xf bank_mask:0xf\n\t"
    "s_waitcnt lgkmcnt(0)\n\t"
    "v_fmac_f32_dpp v40, v60, v38 quad_perm:[3,3,3,3] row_mask:0xf bank_mask:0xf\n\t"
    "v_fmac_f32_dpp v41, v60, v39 quad_perm:[3,3,3,3] row_mask:0xf bank_mask:0xf\n\t"
    "v_cvt_pk_bf16_f32 v60, v40, v41\n\t"
    "v_min_u32 v60, -2, v60\n\t"
    "s_waitcnt vmcnt(0)\n\t"
    "v_lshrrev_b32 v59, 2, v29\n\t"
    "v_lshrrev_b32 v58, 4, v59\n\t"
    "v_lshlrev_b32 v57, 2, v58\n\t"
    "v_xor_b32 v57, v57, v59\n\t"
    "v_and_b32 v57, 15, v57\n\t"
    "v_lshlrev_b32 v57, 4, v57\n\t"
    "v_lshl_add_u32 v57, v58, 8, v57\n\t"
    "s_add_u32 m0, %[ldsw], 0\n\t"
    "s_add_u32 s90, s36, 0\n\t"
    "buffer_load_dwordx4 v57, s[64:67], s90 offen nt lds\n\t"
    "v_xor_b32 v58, 16, v57\n\t"
    "buffer_load_dwordx4 v58, s[64:67], s90 offen offset:1024 nt lds\n\t"
    "v_xor_b32 v58, 32, v57\n\t"
    "buffer_load_dwordx4 v58, s[64:67], s90 offen offset:2048 nt lds\n\t"
    "v_xor_b32 v58, 48, v57\n\t"
    "buffer_load_dwordx4 v58, s[64:67], s90 offen offset:3072 nt lds\n\t"
    "s_add_u32 m0, %[ldsw], 4096\n\t"
    "s_add_u32 s90, s36, 16384\n\t"
    "buffer_load_dwordx4 v57, s[64:67], s90 offen nt lds\n\t"
    "v_xor_b32 v58, 16, v57\n\t"
    "buffer_load_dwordx4 v58, s[64:67], s90 offen offset:1024 nt lds\n\t"
    "v_xor_b32 v58, 32, v57\n\t"
    "buffer_load_dwordx4 v58, s[64:67], s90 offen offset:2048 nt lds\n\t"
    "v_xor_b32 v58, 48, v57\n\t"
    "buffer_load_dwordx4 v58, s[64:67], s90 offen offset:3072 nt lds\n\t"
    "s_memrealtime s[86:87]\n\t"
    "v_lshlrev_b32 v0, 2, v29\n\t"
    "v_mov_b32 v8, -1\n\t"
    "v_mov_b32 v9, -1\n\t"
    "v_mov_b32 v10, -1\n\t"
    "v_mov_b32 v11, -1\n\t"
    "s_waitcnt lgkmcnt(0)\n\t"
    TP(16)
    STAMP2_STR
    "s_mov_b32 s88, s38\n\t"
    "87:\n\t"
    "s_sub_u32 s89, s86, s88\n\t"
    "s_cmp_le_i32 s89, " STR1(SLACK) "\n\t"
    "s_cbranch_scc1 86f\n\t"
    "s_add_u32 s88, s88, " STR1(GTICK) "\n\t"
    "s_bitset1_b32 s75, 31\n\t"
    "s_branch 87b\n\t"
    "86:\n\t"
    "s_memrealtime s[86:87]\n\t"
    "s_waitcnt lgkmcnt(0)\n\t"
    "s_sub_u32 s86, s86, s88\n\t"
    "s_cmp_lt_i32 s86, 0\n\t"
    "s_cbranch_scc1 86b\n\t"
    "buffer_store_dword v60, v29, %[rws], s31 offen sc1\n\t"
    "buffer_store_dword v61, v30, %[rws], s32 offen sc1\n\t"
    "s_memtime s[78:79]\n\t"
    "buffer_store_dword v12, v29, %[rws], s33 offen sc1\n\t"
    "buffer_store_dword v13, v30, %[rws], s34 offen sc1\n\t"
    "s_bitcmp1_b32 %[ldsw], 12\n\t"
    "s_cselect_b32 exec_hi, 0xffff, -1\n\t"
    "buffer_store_dwordx4 v[8:11], v0, %[rws], s37 offen sc1\n\t"
    "s_mov_b64 exec, -1\n\t"
    "s_nop 0\n\t"
    "s_branch 99f\n\t"
    "84:\n\t"
    "s_add_u32 s92, s92, 1\n\t"
    "s_cmp_gt_u32 s92, 16384\n\t"
    "s_cbranch_scc1 83b\n\t"
    "s_sleep 1\n\t"
    "s_branch 85f\n\t"
    "85:\n\t"
    "buffer_load_dwordx4 v[8:11], v51, %[rws], s30 offen sc1\n\t"
    "s_waitcnt vmcnt(0)\n\t"
    "s_branch 82b\n\t"
    "14:\n\t"
    TP(2)
    "s_bitcmp1_b32 s25, 0\n\t"
    "s_cbranch_scc0 15b\n\t"
    "s_memrealtime s[82:83]\n\t"
    "v_lshrrev_b32 v17, 4, v0\n\t"
    "v_lshlrev_b32 v7, 2, v17\n\t"
    "v_xor_b32 v7, v7, v0\n\t"
    "v_and_b32 v7, 15, v7\n\t"
    "v_lshlrev_b32 v7, 4, v7\n\t"
    "v_lshl_add_u32 v7, v17, 8, v7\n\t"
    "s_bitcmp1_b32 %[outw], 11\n\t"
    "s_cbranch_scc1 17f\n\t"
    "s_add_u32 m0, %[ldsw], 8192\n\t"
    "s_add_u32 s90, s35, 32768\n\t"
    "buffer_load_dwordx4 v7, s[52:55], s90 offen nt lds\n\t"
    "v_xor_b32 v17, 16, v7\n\t"
    "buffer_load_dwordx4 v17, s[52:55], s90 offen offset:1024 nt lds\n\t"
    "v_xor_b32 v17, 32, v7\n\t"
    "buffer_load_dwordx4 v17, s[52:55], s90 offen offset:2048 nt lds\n\t"
    "v_xor_b32 v17, 48, v7\n\t"
    "buffer_load_dwordx4 v17, s[52:55], s90 offen offset:3072 nt lds\n\t"
    "17:\n\t"
    "v_lshrrev_b32 v17, 4, v0\n\t"
    "v_xor_b32 v17, v17, v0\n\t"
    "v_and_b32 v17, 3, v17\n\t"
    "v_lshlrev_b32 v6, 6, v17\n\t"
    "v_bfe_u32 v17, v0, 2, 2\n\t"
    "v_lshl_add_u32 v6, v17, 4, v6\n\t"
    "v_and_b32 v17, 15, v0\n\t"
    "v_lshl_add_u32 v6, v17, 8, v6\n\t"
    "v_add_u32 v6, %[ldsw], v6\n\t"
    "ds_read_b128 a[128:131], v6 offset:0\n\t"
    "v_xor_b32 v2, 16, v6\n\t"
    "ds_read_b128 a[132:135], v2 offset:0\n\t"
    "v_xor_b32 v3, 32, v6\n\t"
    "ds_read_b128 a[136:139], v3 offset:0\n\t"
    "v_xor_b32 v2, 48, v6\n\t"
    "ds_read_b128 a[140:143], v2 offset:0\n\t"
    "ds_read_b128 a[144:147], v6 offset:4096\n\t"
    "v_xor_b32 v2, 16, v6\n\t"
    "ds_read_b128 a[148:151], v2 offset:4096\n\t"
    "v_xor_b32 v3, 32, v6\n\t"
    "ds_read_b128 a[152:155], v3 offset:4096\n\t"
    "v_xor_b32 v2, 48, v6\n\t"
    "ds_read_b128 a[156:159], v2 offset:4096\n\t"
    "s_waitcnt lgkmcnt(4)\n\t"
    "s_add_u32 m0, %[ldsw], 0\n\t"
    "s_add_u32 s90, s35, 49152\n\t"
    "buffer_load_dwordx4 v7, s[52:55], s90 offen nt lds\n\t"
    "v_xor_b32 v2, 16, v7\n\t"
    "buffer_load_dwordx4 v2, s[52:55], s90 offen offset:1024 nt lds\n\t"
    "v_xor_b32 v2, 32, v7\n\t"
    "buffer_load_dwordx4 v2, s[52:55], s90 offen offset:2048 nt lds\n\t"
    "v_xor_b32 v2, 48, v7\n\t"
    "buffer_load_dwordx4 v2, s[52:55], s90 offen offset:3072 nt lds\n\t"
    "s_waitcnt lgkmcnt(0)\n\t"
    "s_add_u32 m0, %[ldsw], 4096\n\t"
    "s_add_u32 s90, s35, 65536\n\t"
    "buffer_load_dwordx4 v7, s[52:55], s90 offen nt lds\n\t"
    "v_xor_b32 v2, 16, v7\n\t"
    "buffer_load_dwordx4 v2, s[52:55], s90 offen offset:1024 nt lds\n\t"
    "v_xor_b32 v2, 32, v7\n\t"
    "buffer_load_dwordx4 v2, s[52:55], s90 offen offset:2048 nt lds\n\t"
    "v_xor_b32 v2, 48, v7\n\t"
    "buffer_load_dwordx4 v2, s[52:55], s90 offen offset:3072 nt lds\n\t"
    "s_and_b32 s90, s24, 1\n\t"
    "s_lshl_b32 s90, s90, 6\n\t"
    "s_bitcmp1_b32 %[outw], 11\n\t"
    "s_cselect_b32 s91, 96, 0\n\t"
    "s_lshl4_add_u32 s91, s24, s91\n\t"
    "s_load_dwordx2 s[68:69], %[ka], s90 offset:24\n\t"
    "s_load_dwordx2 s[74:75], %[ka], s91 offset:168\n\t"
    "s_sub_u32 s90, %[clus], %[mine]\n\t"
    "s_add_u32 s42, s31, s90\n\t"
    "s_add_u32 s43, s32, s90\n\t"
    "s_lshl_b32 s90, %[w], 8\n\t"
    "s_sub_u32 s90, s31, s90\n\t"
    "s_add_u32 s44, s90, 1536\n\t"
    "s_and_b32 s90, s24, 6\n\t"
    "s_cmp_eq_u32 s90, 2\n\t"
    "s_cselect_b32 s40, 3584, 0\n\t"
    "s_cmp_lt_u32 s24, 4\n\t"
    "s_cselect_b32 s90, 12288, 0\n\t"
    "s_add_u32 s80, %[ldsw], s90\n\t"
    "s_cmp_gt_u32 s24, 2\n\t"
    "s_cselect_b32 s93, 1, 0\n\t"
    "s_cselect_b32 s46, 4, 68\n\t"
    "s_cmp_eq_u32 s24, 2\n\t"
    "s_cselect_b32 s46, 20, s46\n\t"
    "s_add_u32 s81, s24, 1\n\t"
    "s_lshl2_add_u32 s90, s93, %[w]\n\t"
    "s_lshl_b32 s90, s90, 9\n\t"
    "s_add_u32 s50, s90, 159552\n\t"
    "s_lshl1_add_u32 s90, s93, s24\n\t"
    "s_mul_i32 s90, s90, 1792\n\t"
    "s_add_u32 s51, s90, %[ybase]\n\t"
    "s_add_u32 s90, s90, %[mine]\n\t"
    "s_add_u32 s76, s90, 1664\n\t"
    "s_mov_b32 s47, 157952\n\t"
    "s_mul_i32 s48, %[w], 384\n\t"
    "s_add_u32 s48, s48, s47\n\t"
    "s_lshl4_add_u32 s49, %[w], 159488\n\t"
    "v_mbcnt_lo_u32_b32 v56, -1, 0\n\t"
    "v_mbcnt_hi_u32_b32 v56, -1, v56\n\t"
    "v_lshrrev_b32 v58, 4, v56\n\t"
    "v_xor_b32 v58, v58, v56\n\t"
    "v_and_b32 v58, 3, v58\n\t"
    "v_lshlrev_b32 v57, 6, v58\n\t"
    "v_bfe_u32 v58, v56, 2, 2\n\t"
    "v_lshl_add_u32 v57, v58, 4, v57\n\t"
    "v_and_b32 v58, 15, v56\n\t"
    "v_lshl_add_u32 v57, v58, 8, v57\n\t"
    "v_add_u32 v57, %[ldsw], v57\n\t"
    "s_waitcnt vmcnt(8)\n\t"
    "ds_read_b128 a[160:163], v57 offset:8192\n\t"
    "v_xor_b32 v58, 16, v57\n\t"
    "ds_read_b128 a[164:167], v58 offset:8192\n\t"
    "v_xor_b32 v59, 32, v57\n\t"
    "ds_read_b128 a[168:171], v59 offset:8192\n\t"
    "v_xor_b32 v58, 48, v57\n\t"
    "ds_read_b128 a[172:175], v58 offset:8192\n\t"
    "v_lshrrev_b32 v58, 4, v56\n\t"
    "v_lshlrev_b32 v57, 2, v58\n\t"
    "v_xor_b32 v57, v57, v56\n\t"
    "v_and_b32 v57, 15, v57\n\t"
    "v_lshlrev_b32 v57, 4, v57\n\t"
    "v_lshl_add_u32 v57, v58, 8, v57\n\t"
    "s_waitcnt lgkmcnt(0)\n\t"
    "s_add_u32 m0, %[ldsw], 8192\n\t"
    "s_add_u32 s90, s35, 81920\n\t"
    "buffer_load_dwordx4 v57, s[52:55], s90 offen nt lds\n\t"
    "v_xor_b32 v58, 16, v57\n\t"
    "buffer_load_dwordx4 v58, s[52:55], s90 offen offset:1024 nt lds\n\t"
    "v_xor_b32 v58, 32, v57\n\t"
    "buffer_load_dwordx4 v58, s[52:55], s90 offen offset:2048 nt lds\n\t"
    "v_xor_b32 v58, 48, v57\n\t"
    "buffer_load_dwordx4 v58, s[52:55], s90 offen offset:3072 nt lds\n\t"
    "70:\n\t"
    "v_mbcnt_lo_u32_b32 v50, -1, 0\n\t"
    "v_mbcnt_hi_u32_b32 v50, -1, v50\n\t"
    "v_lshrrev_b32 v48, 4, v50\n\t"
    "v_xor_b32 v48, v48, v50\n\t"
    "v_and_b32 v48, 3, v48\n\t"
    "v_lshlrev_b32 v45, 6, v48\n\t"
    "v_bfe_u32 v48, v50, 2, 2\n\t"
    "v_lshl_add_u32 v45, v48, 4, v45\n\t"
    "v_and_b32 v48, 15, v50\n\t"
    "v_lshl_add_u32 v45, v48, 8, v45\n\t"
    "v_add_u32 v45, %[ldsw], v45\n\t"
    "v_lshrrev_b32 v48, 4, v50\n\t"
    "v_lshlrev_b32 v44, 2, v48\n\t"
    "v_xor_b32 v44, v44, v50\n\t"
    "v_and_b32 v44, 15, v44\n\t"
    "v_lshlrev_b32 v44, 4, v44\n\t"
    "v_lshl_add_u32 v44, v48, 8, v44\n\t"
    "s_waitcnt vmcnt(8)\n\t"
    TP(23)
    "ds_read_b128 a[176:179], v45 offset:0\n\t"
    "v_xor_b32 v48, 16, v45\n\t"
    "ds_read_b128 a[180:183], v48 offset:0\n\t"
    "v_xor_b32 v49, 32, v45\n\t"
    "ds_read_b128 a[184:187], v49 offset:0\n\t"
    "v_xor_b32 v48, 48, v45\n\t"
    "ds_read_b128 a[188:191], v48 offset:0\n\t"
    "s_waitcnt lgkmcnt(0)\n\t"
    "s_add_u32 m0, %[ldsw], 0\n\t"
    "s_add_u32 s90, s35, 98304\n\t"
    "buffer_load_dwordx4 v44, s[52:55], s90 offen nt lds\n\t"
    "v_xor_b32 v48, 16, v44\n\t"
    "buffer_load_dwordx4 v48, s[52:55], s90 offen offset:1024 nt lds\n\t"
    "v_xor_b32 v48, 32, v44\n\t"
    "buffer_load_dwordx4 v48, s[52:55], s90 offen offset:2048 nt lds\n\t"
    "v_xor_b32 v48, 48, v44\n\t"
    "buffer_load_dwordx4 v48, s[52:55], s90 offen offset:3072 nt lds\n\t"
    "v_lshrrev_b32 v52, 4, v50\n\t"
    "v_lshlrev_b32 v47, 1, v52\n\t"
    "v_xor_b32 v47, v47, v50\n\t"
    "v_and_b32 v47, 15, v47\n\t"
    "v_lshlrev_b32 v47, 4, v47\n\t"
    "v_lshl_add_u32 v47, v52, 8, v47\n\t"
    "v_lshrrev_b32 v52, 2, v50\n\t"
    "v_lshlrev_b32 v31, 8, v52\n\t"
    "v_and_b32 v52, 7, v52\n\t"
    "v_lshl_add_u32 v31, v52, 5, v31\n\t"
    "v_and_b32 v52, 3, v50\n\t"
    "v_lshl_add_u32 v31, v52, 3, v31\n\t"
    "v_add_u32 v31, %[ldsw], v31\n\t"
    "v_and_b32 v51, 3, v50\n\t"
    "v_lshrrev_b32 v52, 4, v50\n\t"
    "v_lshl_add_u32 v51, v51, 2, v52\n\t"
    "v_mul_u32_u24 v51, 14592, v51\n\t"
    "v_and_b32 v52, 12, v50\n\t"
    "v_lshl_add_u32 v51, v52, 2, v51\n\t"
    "s_mov_b32 m0, s77\n\t"
    "s_lshl_b32 exec_lo, 0x11111111, %[w]\n\t"
    "s_mov_b32 exec_hi, exec_lo\n\t"
    "buffer_load_dwordx4 v51, %[rws], s30 offen sc1 lds\n\t"
    "s_mov_b64 exec, -1\n\t"
    "s_waitcnt vmcnt(9)\n\t"
    TP(24)
    "ds_read_b128 a[192:195], v45 offset:4096\n\t"
    "v_xor_b32 v48, 16, v45\n\t"
    "ds_read_b128 a[196:199], v48 offset:4096\n\t"
    "v_xor_b32 v49, 32, v45\n\t"
    "ds_read_b128 a[200:203], v49 offset:4096\n\t"
    "v_xor_b32 v48, 48, v45\n\t"
    "ds_read_b128 a[204:207], v48 offset:4096\n\t"
    "s_waitcnt lgkmcnt(0)\n\t"
    "s_add_u32 m0, %[ldsw], 4096\n\t"
    "s_add_u32 s90, s35, 114688\n\t"
    "buffer_load_dwordx4 v44, s[52:55], s90 offen nt lds\n\t"
    "v_xor_b32 v48, 16, v44\n\t"
    "buffer_load_dwordx4 v48, s[52:55], s90 offen offset:1024 nt lds\n\t"
    "v_xor_b32 v48, 32, v44\n\t"
    "buffer_load_dwordx4 v48, s[52:55], s90 offen offset:2048 nt lds\n\t"
    "v_xor_b32 v48, 48, v44\n\t"
    "buffer_load_dwordx4 v48, s[52:55], s90 offen offset:3072 nt lds\n\t"
    "v_and_b32 v5, 3, v50\n\t"
    "v_mul_u32_u24 v5, 528, v5\n\t"
    "v_add_u32 v5, s26, v5\n\t"
    "v_and_b32 v52, 12, v50\n\t"
    "v_lshl_add_u32 v4, v52, 4, v5\n\t"
    "v_and_b32 v52, 48, v50\n\t"
    "v_add_u32 v4, v4, v52\n\t"
    "v_lshl_add_u32 v28, v50, 3, s27\n\t"
    "v_lshlrev_b32 v30, 24, v50\n\t"
    "v_lshlrev_b32 v29, 2, v50\n\t"
    "s_mov_b32 s84, 0xf000f0\n\t"
    "s_mov_b32 s85, 0xf000f0\n\t"
    "s_mov_b32 s86, 0xf000f00\n\t"
    "s_mov_b32 s87, 0xf000f00\n\t"
    "s_mov_b32 s88, 0xf000f000\n\t"
    "s_mov_b32 s89, 0xf000f000\n\t"
    "v_mov_b32 v0, 0x3f803f80\n\t"
    "v_mov_b32 v1, 0x3f803f80\n\t"
    "v_mov_b32 v2, 0x3f803f80\n\t"
    "v_mov_b32 v3, 0x3f803f80\n\t"
    "s_mov_b32 s92, 0\n\t"
    "s_add_u32 s38, s38, s72\n\t"
    "s_add_u32 s90, s82, s39\n\t"
    "94:\n\t"
    "s_sub_u32 s91, s90, s38\n\t"
    "s_cmp_gt_i32 s91, 0\n\t"
    "s_cbranch_scc0 93f\n\t"
    "s_add_u32 s38, s38, " STR1(GTICK) "\n\t"
    "s_branch 94b\n\t"
    "93:\n\t"
    "s_waitcnt vmcnt(9)\n\t"
    TP(25)
    "ds_read_b128 a[208:211], v45 offset:8192\n\t"
    "v_xor_b32 v48, 16, v45\n\t"
    "ds_read_b128 a[212:215], v48 offset:8192\n\t"
    "v_xor_b32 v49, 32, v45\n\t"
    "ds_read_b128 a[216:219], v49 offset:8192\n\t"
    "v_xor_b32 v48, 48, v45\n\t"
    "ds_read_b128 a[220:223], v48 offset:8192\n\t"
    "s_waitcnt lgkmcnt(0)\n\t"
    "s_add_u32 m0, %[ldsw], 28672\n\t"
    "s_add_u32 s90, s35, 0\n\t"
    "v_xor_b32 v48, 128, v47\n\t"
    "buffer_load_dwordx4 v47, s[56:59], s90 offen offset:0 nt lds\n\t"
    "buffer_load_dwordx4 v48, s[56:59], s90 offen offset:1024 nt lds\n\t"
    "buffer_load_dwordx4 v47, s[56:59], s90 offen offset:2048 nt lds\n\t"
    "buffer_load_dwordx4 v48, s[56:59], s90 offen offset:3072 nt lds\n\t"
    "s_waitcnt vmcnt(8)\n\t"
    TP(26)
    "ds_read_b128 a[224:227], v45 offset:0\n\t"
    "v_xor_b32 v48, 16, v45\n\t"
    "ds_read_b128 a[228:231], v48 offset:0\n\t"
    "v_xor_b32 v49, 32, v45\n\t"
    "ds_read_b128 a[232:235], v49 offset:0\n\t"
    "v_xor_b32 v48, 48, v45\n\t"
    "ds_read_b128 a[236:239], v48 offset:0\n\t"
    "s_waitcnt lgkmcnt(0)\n\t"
    "s_add_u32 m0, %[ldsw], 32768\n\t"
    "s_add_u32 s90, s35, 16384\n\t"
    "v_xor_b32 v48, 128, v47\n\t"
    "buffer_load_dwordx4 v47, s[56:59], s90 offen offset:0 nt lds\n\t"
    "buffer_load_dwordx4 v48, s[56:59], s90 offen offset:1024 nt lds\n\t"
    "buffer_load_dwordx4 v47, s[56:59], s90 offen offset:2048 nt lds\n\t"
    "buffer_load_dwordx4 v48, s[56:59], s90 offen offset:3072 nt lds\n\t"
    "v_lshl_add_u32 v52, v50, 4, s77\n\t"
    "s_barrier\n\t"
    "ds_read_b128 v[8:11], v52\n\t"
    "s_waitcnt lgkmcnt(0)\n\t"
    "72:\n\t"
    "v_max3_u32 v52, v8, v9, v10\n\t"
    "v_max_u32 v52, v52, v11\n\t"
    "v_cmp_eq_u32 vcc, -1, v52\n\t"
    "s_cbranch_vccnz 74f\n\t"
    "73:\n\t"
    "v_mov_b32_dpp v12, v8 row_ror:12 row_mask:0xf bank_mask:0xf\n\t"
    "v_mov_b32_dpp v13, v9 row_ror:12 row_mask:0xf bank_mask:0xf\n\t"
    "v_mov_b32_dpp v14, v10 row_ror:12 row_mask:0xf bank_mask:0xf\n\t"
    "v_mov_b32_dpp v15, v11 row_ror:12 row_mask:0xf bank_mask:0xf\n\t"
    "v_mov_b32_dpp v16, v8 row_ror:8 row_mask:0xf bank_mask:0xf\n\t"
    "v_mov_b32_dpp v17, v9 row_ror:8 row_mask:0xf bank_mask:0xf\n\t"
    "v_mov_b32_dpp v18, v10 row_ror:8 row_mask:0xf bank_mask:0xf\n\t"
    "v_mov_b32_dpp v19, v11 row_ror:8 row_mask:0xf bank_mask:0xf\n\t"
    "v_mov_b32_dpp v20, v8 row_ror:4 row_mask:0xf bank_mask:0xf\n\t"
    "v_mov_b32_dpp v21, v9 row_ror:4 row_mask:0xf bank_mask:0xf\n\t"
    "v_mov_b32_dpp v22, v10 row_ror:4 row_mask:0xf bank_mask:0xf\n\t"
    "v_mov_b32_dpp v23, v11 row_ror:4 row_mask:0xf bank_mask:0xf\n\t"
    "s_nop 1\n\t"
    "v_mfma_f32_16x16x32_bf16 v[56:59], a[128:131], v[8:11], 0\n\t"
    "v_mfma_f32_16x16x32_bf16 v[56:59], a[132:135], v[12:15], v[56:59]\n\t"
    "v_mfma_f32_16x16x32_bf16 v[56:59], a[136:139], v[16:19], v[56:59]\n\t"
    "v_mfma_f32_16x16x32_bf16 v[56:59], a[140:143], v[20:23], v[56:59]\n\t"
    "v_mfma_f32_16x16x32_bf16 v[60:63], a[144:147], v[8:11], 0\n\t"
    "v_mfma_f32_16x16x32_bf16 v[60:63], a[148:151], v[12:15], v[60:63]\n\t"
    "v_mfma_f32_16x16x32_bf16 v[60:63], a[152:155], v[16:19], v[60:63]\n\t"
    "v_mfma_f32_16x16x32_bf16 v[60:63], a[156:159], v[20:23], v[60:63]\n\t"
    "v_mfma_f32_16x16x32_bf16 v[32:35], a[164:167], v[8:11], 0\n\t"
    "v_mfma_f32_16x16x32_bf16 v[32:35], a[168:171], v[12:15], v[32:35]\n\t"
    "v_mfma_f32_16x16x32_bf16 v[32:35], a[172:175], v[16:19], v[32:35]\n\t"
    "v_mfma_f32_16x16x32_bf16 v[32:35], a[160:163], v[20:23], v[32:35]\n\t"
    "s_waitcnt vmcnt(8)\n\t"
    TP(27)
    "ds_read_b128 a[240:243], v45 offset:4096\n\t"
    "v_xor_b32 v48, 16, v45\n\t"
    "ds_read_b128 a[244:247], v48 offset:4096\n\t"
    "v_xor_b32 v49, 32, v45\n\t"
    "ds_read_b128 a[248:251], v49 offset:4096\n\t"
    "v_xor_b32 v48, 48, v45\n\t"
    "ds_read_b128 a[252:255], v48 offset:4096\n\t"
    "s_waitcnt lgkmcnt(0)\n\t"
    "s_add_u32 m0, %[ldsw], 0\n\t"
    "s_add_u32 s90, s35, 32768\n\t"
    "v_xor_b32 v48, 128, v47\n\t"
    "buffer_load_dwordx4 v47, s[56:59], s90 offen offset:0 nt lds\n\t"
    "buffer_load_dwordx4 v48, s[56:59], s90 offen offset:1024 nt lds\n\t"
    "buffer_load_dwordx4 v47, s[56:59], s90 offen offset:2048 nt lds\n\t"
    "buffer_load_dwordx4 v48, s[56:59], s90 offen offset:3072 nt lds\n\t"
    "v_mfma_f32_16x16x32_bf16 v[36:39], a[180:183], v[8:11], 0\n\t"
    "s_nop 0\n\t"
    "v_mfma_f32_16x16x32_bf16 v[36:39], a[184:187], v[12:15], v[36:39]\n\t"
    "s_nop 0\n\t"
    "v_mfma_f32_16x16x32_bf16 v[36:39], a[188:191], v[16:19], v[36:39]\n\t"
    "s_nop 0\n\t"
    "v_mfma_f32_16x16x32_bf16 v[36:39], a[176:179], v[20:23], v[36:39]\n\t"
    "s_nop 0\n\t"
    "v_mfma_f32_16x16x32_bf16 v[40:43], a[200:203], v[8:11], 0\n\t"
    "v_cndmask_b32_e64 v56, v56, v32, s[84:85]\n\t"
    "v_mfma_f32_16x16x32_bf16 v[40:43], a[204:207], v[12:15], v[40:43]\n\t"
    "v_cndmask_b32_e64 v57, v57, v33, s[84:85]\n\t"
    "v_mfma_f32_16x16x32_bf16 v[40:43], a[192:195], v[16:19], v[40:43]\n\t"
    "v_cndmask_b32_e64 v58, v58, v34, s[84:85]\n\t"
    "v_mfma_f32_16x16x32_bf16 v[40:43], a[196:199], v[20:23], v[40:43]\n\t"
    "v_cndmask_b32_e64 v59, v59, v35, s[84:85]\n\t"
    "v_mfma_f32_16x16x32_bf16 v[32:35], a[216:219], v[8:11], 0\n\t"
    "v_cndmask_b32_e64 v60, v60, v36, s[84:85]\n\t"
    "v_mfma_f32_16x16x32_bf16 v[32:35], a[220:223], v[12:15], v[32:35]\n\t"
    "v_cndmask_b32_e64 v61, v61, v37, s[84:85]\n\t"
    "v_mfma_f32_16x16x32_bf16 v[32:35], a[208:211], v[16:19], v[32:35]\n\t"
    "v_cndmask_b32_e64 v62, v62, v38, s[84:85]\n\t"
    "v_mfma_f32_16x16x32_bf16 v[32:35], a[212:215], v[20:23], v[32:35]\n\t"
    "v_cndmask_b32_e64 v63, v63, v39, s[84:85]\n\t"
    "v_mfma_f32_16x16x32_bf16 v[36:39], a[236:239], v[8:11], 0\n\t"
    "v_cndmask_b32_e64 v56, v56, v40, s[86:87]\n\t"
    "v_mfma_f32_16x16x32_bf16 v[36:39], a[224:227], v[12:15], v[36:39]\n\t"
    "v_cndmask_b32_e64 v57, v57, v41, s[86:87]\n\t"
    "v_mfma_f32_16x16x32_bf16 v[36:39], a[228:231], v[16:19], v[36:39]\n\t"
    "v_cndmask_b32_e64 v58, v58, v42, s[86:87]\n\t"
    "v_mfma_f32_16x16x32_bf16 v[36:39], a[232:235], v[20:23], v[36:39]\n\t"
    "v_cndmask_b32_e64 v59, v59, v43, s[86:87]\n\t"
    "v_mfma_f32_16x16x32_bf16 v[40:43], a[252:255], v[8:11], 0\n\t"
    "v_cndmask_b32_e64 v60, v60, v32, s[86:87]\n\t"
    "v_mfma_f32_16x16x32_bf16 v[40:43], a[240:243], v[12:15], v[40:43]\n\t"
    "v_cndmask_b32_e64 v61, v61, v33, s[86:87]\n\t"
    "v_mfma_f32_16x16x32_bf16 v[40:43], a[244:247], v[16:19], v[40:43]\n\t"
    "v_cndmask_b32_e64 v62, v62, v34, s[86:87]\n\t"
    "v_mfma_f32_16x16x32_bf16 v[40:43], a[248:251], v[20:23], v[40:43]\n\t"
    "v_cndmask_b32_e64 v63, v63, v35, s[86:87]\n\t"
    "s_waitcnt vmcnt(8)\n\t"
    TP(28)
    "s_add_u32 m0, %[ldsw], 4096\n\t"
    "s_add_u32 s90, s35, 49152\n\t"
    "v_xor_b32 v48, 128, v47\n\t"
    "buffer_load_dwordx4 v47, s[56:59], s90 offen offset:0 nt lds\n\t"
    "buffer_load_dwordx4 v48, s[56:59], s90 offen offset:1024 nt lds\n\t"
    "buffer_load_dwordx4 v47, s[56:59], s90 offen offset:2048 nt lds\n\t"
    "buffer_load_dwordx4 v48, s[56:59], s90 offen offset:3072 nt lds\n\t"
    "v_cndmask_b32_e64 v56, v56, v36, s[88:89]\n\t"
    "v_cndmask_b32_e64 v57, v57, v37, s[88:89]\n\t"
    "v_cndmask_b32_e64 v58, v58, v38, s[88:89]\n\t"
    "v_cndmask_b32_e64 v59, v59, v39, s[88:89]\n\t"
    "v_max3_f32 v6, v56, v57, v58\n\t"
    "v_max_f32 v6, v6, v59\n\t"
    "s_nop 1\n\t"
    "v_cndmask_b32_e64 v60, v60, v40, s[88:89]\n\t"
    "v_cndmask_b32_e64 v61, v61, v41, s[88:89]\n\t"
    "v_cndmask_b32_e64 v62, v62, v42, s[88:89]\n\t"
    "v_cndmask_b32_e64 v63, v63, v43, s[88:89]\n\t"
    "v_max3_f32 v6, v6, v60, v61\n\t"
    "v_max3_f32 v6, v6, v62, v63\n\t"
    "s_nop 1\n\t"
    "v_max_f32_dpp v6, v6, v6 row_ror:4 row_mask:0xf bank_mask:0xf\n\t"
    "s_nop 1\n\t"
    "v_max_f32_dpp v6, v6, v6 row_ror:8 row_mask:0xf bank_mask:0xf\n\t"
    "v_mov_b32 v46, v6\n\t"
    "s_nop 1\n\t"
    "v_permlane16_swap_b32 v6, v46\n\t"
    "v_max_f32 v6, v6, v46\n\t"
    "v_mov_b32 v46, v6\n\t"
    "s_nop 1\n\t"
    "v_permlane32_swap_b32 v6, v46\n\t"
    "v_max_f32 v6, v6, v46\n\t"
    "v_mul_f32 v6, s28, v6\n\t"
    "v_fma_f32 v56, v56, s28, -v6\n\t"
    "v_fma_f32 v57, v57, s28, -v6\n\t"
    "v_fma_f32 v58, v58, s28, -v6\n\t"
    "v_fma_f32 v59, v59, s28, -v6\n\t"
    "v_fma_f32 v60, v60, s28, -v6\n\t"
    "v_fma_f32 v61, v61, s28, -v6\n\t"
    "v_fma_f32 v62, v62, s28, -v6\n\t"
    "v_fma_f32 v63, v63, s28, -v6\n\t"
    "v_exp_f32 v56, v56\n\t"
    "v_exp_f32 v57, v57\n\t"
    "v_exp_f32 v58, v58\n\t"
    "v_exp_f32 v59, v59\n\t"
    "v_exp_f32 v60, v60\n\t"
    "v_exp_f32 v61, v61\n\t"
    "v_exp_f32 v62, v62\n\t"
    "v_exp_f32 v63, v63\n\t"
    "v_cvt_pk_bf16_f32 v8, v56, v57\n\t"
    "v_cvt_pk_bf16_f32 v9, v58, v59\n\t"
    "v_cvt_pk_bf16_f32 v10, v60, v61\n\t"
    "v_cvt_pk_bf16_f32 v11, v62, v63\n\t"
    "s_nop 1\n\t"
    "v_mfma_f32_16x16x32_bf16 v[56:59], v[0:3], v[8:11], 0\n\t"
    "v_mov_b32_dpp v12, v8 row_ror:12 row_mask:0xf bank_mask:0xf\n\t"
    "v_mov_b32_dpp v13, v9 row_ror:12 row_mask:0xf bank_mask:0xf\n\t"
    "v_mov_b32_dpp v14, v10 row_ror:12 row_mask:0xf bank_mask:0xf\n\t"
    "v_mov_b32_dpp v15, v11 row_ror:12 row_mask:0xf bank_mask:0xf\n\t"
    "v_mov_b32_dpp v16, v8 row_ror:8 row_mask:0xf bank_mask:0xf\n\t"
    "v_mov_b32_dpp v17, v9 row_ror:8 row_mask:0xf bank_mask:0xf\n\t"
    "v_mov_b32_dpp v18, v10 row_ror:8 row_mask:0xf bank_mask:0xf\n\t"
    "v_mov_b32_dpp v19, v11 row_ror:8 row_mask:0xf bank_mask:0xf\n\t"
    "v_add_f32_dpp v7, v56, v56 row_ror:4 row_mask:0xf bank_mask:0xf\n\t"
    "v_mov_b32 v57, v47\n\t"
    "s_nop 0\n\t"
    "v_add_f32_dpp v7, v7, v7 row_ror:8 row_mask:0xf bank_mask:0xf\n\t"
    "ds_write_b64 v5, v[6:7] offset:512\n\t"
    "s_waitcnt vmcnt(8)\n\t"
    TP(29)
    "s_add_u32 m0, %[ldsw], 8192\n\t"
    "s_add_u32 s90, s35, 65536\n\t"
    "v_xor_b32 v58, 128, v57\n\t"
    "buffer_load_dwordx4 v57, s[56:59], s90 offen offset:0 nt lds\n\t"
    "buffer_load_dwordx4 v58, s[56:59], s90 offen offset:1024 nt lds\n\t"
    "buffer_load_dwordx4 v57, s[56:59], s90 offen offset:2048 nt lds\n\t"
    "buffer_load_dwordx4 v58, s[56:59], s90 offen offset:3072 nt lds\n\t"
    "ds_read_b64_tr_b16 v[32:33], v31 offset:28672\n\t"
    "ds_read_b64_tr_b16 v[34:35], v31 offset:32768\n\t"
    "v_xor_b32 v59, 32, v31\n\t"
    "ds_read_b64_tr_b16 v[36:37], v59 offset:28672\n\t"
    "ds_read_b64_tr_b16 v[38:39], v59 offset:32768\n\t"
    "v_xor_b32 v58, 64, v31\n\t"
    "ds_read_b64_tr_b16 v[40:41], v58 offset:28672\n\t"
    "ds_read_b64_tr_b16 v[42:43], v58 offset:32768\n\t"
    "v_xor_b32 v59, 96, v31\n\t"
    "ds_read_b64_tr_b16 v[44:45], v59 offset:28672\n\t"
    "ds_read_b64_tr_b16 v[46:47], v59 offset:32768\n\t"
    "v_xor_b32 v58, 128, v31\n\t"
    "ds_read_b64_tr_b16 v[48:49], v58 offset:28672\n\t"
    "ds_read_b64_tr_b16 v[50:51], v58 offset:32768\n\t"
    "v_xor_b32 v59, 160, v31\n\t"
    "ds_read_b64_tr_b16 v[52:53], v59 offset:28672\n\t"
    "ds_read_b64_tr_b16 v[54:55], v59 offset:32768\n\t"
    "v_xor_b32 v58, 192, v31\n\t"
    "ds_read_b64_tr_b16 v[20:21], v58 offset:28672\n\t"
    "ds_read_b64_tr_b16 v[22:23], v58 offset:32768\n\t"
    "v_xor_b32 v59, 224, v31\n\t"
    "ds_read_b64_tr_b16 v[24:25], v59 offset:28672\n\t"
    "ds_read_b64_tr_b16 v[26:27], v59 offset:32768\n\t"
    "s_waitcnt lgkmcnt(0)\n\t"
    "v_mfma_f32_16x16x32_bf16 v[0:3], v[32:35], v[8:11], 0\n\t"
    "v_mfma_f32_16x16x32_bf16 v[32:35], v[36:39], v[8:11], 0\n\t"
    "v_mfma_f32_16x16x32_bf16 v[36:39], v[40:43], v[8:11], 0\n\t"
    "v_mfma_f32_16x16x32_bf16 v[40:43], v[44:47], v[8:11], 0\n\t"
    "v_mfma_f32_16x16x32_bf16 v[44:47], v[48:51], v[8:11], 0\n\t"
    "v_mfma_f32_16x16x32_bf16 v[48:51], v[52:55], v[8:11], 0\n\t"
    "v_mfma_f32_16x16x32_bf16 v[52:55], v[20:23], v[8:11], 0\n\t"
    "v_mfma_f32_16x16x32_bf16 v[20:23], v[24:27], v[8:11], 0\n\t"
    "s_waitcnt vmcnt(8)\n\t"
    TP(30)
    "ds_read_b64_tr_b16 v[160:161], v31 offset:0\n\t"
    "v_xor_b32 v58, 32, v31\n\t"
    "ds_read_b64_tr_b16 v[164:165], v58 offset:0\n\t"
    "v_xor_b32 v59, 64, v31\n\t"
    "ds_read_b64_tr_b16 v[168:169], v59 offset:0\n\t"
    "v_xor_b32 v58, 96, v31\n\t"
    "ds_read_b64_tr_b16 v[172:173], v58 offset:0\n\t"
    "v_xor_b32 v59, 128, v31\n\t"
    "ds_read_b64_tr_b16 v[176:177], v59 offset:0\n\t"
    "v_xor_b32 v58, 160, v31\n\t"
    "ds_read_b64_tr_b16 v[180:181], v58 offset:0\n\t"
    "v_xor_b32 v59, 192, v31\n\t"
    "ds_read_b64_tr_b16 v[184:185], v59 offset:0\n\t"
    "v_xor_b32 v58, 224, v31\n\t"
    "ds_read_b64_tr_b16 v[188:189], v58 offset:0\n\t"
    "s_waitcnt lgkmcnt(0)\n\t"
    "s_add_u32 m0, %[ldsw], 0\n\t"
    "s_add_u32 s90, s35, 81920\n\t"
    "v_xor_b32 v58, 128, v57\n\t"
    "buffer_load_dwordx4 v57, s[56:59], s90 offen offset:0 nt lds\n\t"
    "buffer_load_dwordx4 v58, s[56:59], s90 offen offset:1024 nt lds\n\t"
    "buffer_load_dwordx4 v57, s[56:59], s90 offen offset:2048 nt lds\n\t"
    "buffer_load_dwordx4 v58, s[56:59], s90 offen offset:3072 nt lds\n\t"
    "s_waitcnt vmcnt(8)\n\t"
    TP(31)
    "ds_read_b64_tr_b16 v[162:163], v31 offset:4096\n\t"
    "v_xor_b32 v58, 32, v31\n\t"
    "ds_read_b64_tr_b16 v[166:167], v58 offset:4096\n\t"
    "v_xor_b32 v59, 64, v31\n\t"
    "ds_read_b64_tr_b16 v[170:171], v59 offset:4096\n\t"
    "v_xor_b32 v58, 96, v31\n\t"
    "ds_read_b64_tr_b16 v[174:175], v58 offset:4096\n\t"
    "v_xor_b32 v59, 128, v31\n\t"
    "ds_read_b64_tr_b16 v[178:179], v59 offset:4096\n\t"
    "v_xor_b32 v58, 160, v31\n\t"
    "ds_read_b64_tr_b16 v[182:183], v58 offset:4096\n\t"
    "v_xor_b32 v59, 192, v31\n\t"
    "ds_read_b64_tr_b16 v[186:187], v59 offset:4096\n\t"
    "v_xor_b32 v58, 224, v31\n\t"
    "ds_read_b64_tr_b16 v[190:191], v58 offset:4096\n\t"
    "s_waitcnt lgkmcnt(0)\n\t"
    "s_add_u32 m0, %[ldsw], 4096\n\t"
    "s_add_u32 s90, s35, 98304\n\t"
    "v_xor_b32 v58, 128, v57\n\t"
    "buffer_load_dwordx4 v57, s[56:59], s90 offen offset:0 nt lds\n\t"
    "buffer_load_dwordx4 v58, s[56:59], s90 offen offset:1024 nt lds\n\t"
    "buffer_load_dwordx4 v57, s[56:59], s90 offen offset:2048 nt lds\n\t"
    "buffer_load_dwordx4 v58, s[56:59], s90 offen offset:3072 nt lds\n\t"
    "v_mfma_f32_16x16x32_bf16 v[0:3], v[160:163], v[12:15], v[0:3]\n\t"
    "v_mov_b32_dpp v24, v8 row_ror:4 row_mask:0xf bank_mask:0xf\n\t"
    "v_mfma_f32_16x16x32_bf16 v[32:35], v[164:167], v[12:15], v[32:35]\n\t"
    "v_mov_b32_dpp v25, v9 row_ror:4 row_mask:0xf bank_mask:0xf\n\t"
    "v_mfma_f32_16x16x32_bf16 v[36:39], v[168:171], v[12:15], v[36:39]\n\t"
    "v_mov_b32_dpp v26, v10 row_ror:4 row_mask:0xf bank_mask:0xf\n\t"
    "v_mfma_f32_16x16x32_bf16 v[40:43], v[172:175], v[12:15], v[40:43]\n\t"
    "v_mov_b32_dpp v27, v11 row_ror:4 row_mask:0xf bank_mask:0xf\n\t"
    "v_mfma_f32_16x16x32_bf16 v[44:47], v[176:179], v[12:15], v[44:47]\n\t"
    "v_and_b32 v62, 12, v29\n\t"
    "v_mfma_f32_16x16x32_bf16 v[48:51], v[180:183], v[12:15], v[48:51]\n\t"
    "v_mul_u32_u24 v62, 528, v62\n\t"
    "v_mfma_f32_16x16x32_bf16 v[52:55], v[184:187], v[12:15], v[52:55]\n\t"
    "v_add_u32 v62, s27, v62\n\t"
    "v_mfma_f32_16x16x32_bf16 v[20:23], v[188:191], v[12:15], v[20:23]\n\t"
    "s_waitcnt vmcnt(8)\n\t"
    TP(32)
    "ds_read_b64_tr_b16 v[192:193], v31 offset:8192\n\t"
    "v_xor_b32 v58, 32, v31\n\t"
    "ds_read_b64_tr_b16 v[196:197], v58 offset:8192\n\t"
    "v_xor_b32 v59, 64, v31\n\t"
    "ds_read_b64_tr_b16 v[200:201], v59 offset:8192\n\t"
    "v_xor_b32 v58, 96, v31\n\t"
    "ds_read_b64_tr_b16 v[204:205], v58 offset:8192\n\t"
    "v_xor_b32 v59, 128, v31\n\t"
    "ds_read_b64_tr_b16 v[208:209], v59 offset:8192\n\t"
    "v_xor_b32 v58, 160, v31\n\t"
    "ds_read_b64_tr_b16 v[212:213], v58 offset:8192\n\t"
    "v_xor_b32 v59, 192, v31\n\t"
    "ds_read_b64_tr_b16 v[216:217], v59 offset:8192\n\t"
    "v_xor_b32 v58, 224, v31\n\t"
    "ds_read_b64_tr_b16 v[220:221], v58 offset:8192\n\t"
    "s_waitcnt lgkmcnt(0)\n\t"
    "s_add_u32 m0, %[ldsw], 8192\n\t"
    "s_add_u32 s90, s35, 114688\n\t"
    "v_xor_b32 v58, 128, v57\n\t"
    "buffer_load_dwordx4 v57, s[56:59], s90 offen offset:0 nt lds\n\t"
    "buffer_load_dwordx4 v58, s[56:59], s90 offen offset:1024 nt lds\n\t"
    "buffer_load_dwordx4 v57, s[56:59], s90 offen offset:2048 nt lds\n\t"
    "buffer_load_dwordx4 v58, s[56:59], s90 offen offset:3072 nt lds\n\t"
    "ds_read_b64 v[12:13], v28\n\t"
    "s_waitcnt lgkmcnt(0)\n\t"
    "s_barrier\n\t"
    "ds_read_b64 v[60:61], v62 offset:512\n\t"
    "s_waitcnt lgkmcnt(0)\n\t"
    "s_nop 1\n\t"
    "v_max_f32_dpp v62, v60, v60 quad_perm:[1,0,3,2] row_mask:0xf bank_mask:0xf\n\t"
    "s_nop 1\n\t"
    "v_max_f32_dpp v62, v62, v62 quad_perm:[2,3,0,1] row_mask:0xf bank_mask:0xf\n\t"
    "v_sub_f32 v60, v60, v62\n\t"
    "v_exp_f32 v60, v60\n\t"
    "s_nop 1\n\t"
    "v_mul_f32 v61, v60, v61\n\t"
    "s_nop 1\n\t"
    "v_add_f32_dpp v63, v61, v61 quad_perm:[1,0,3,2] row_mask:0xf bank_mask:0xf\n\t"
    "s_nop 1\n\t"
    "v_add_f32_dpp v63, v63, v63 quad_perm:[2,3,0,1] row_mask:0xf bank_mask:0xf\n\t"
    "v_log_f32 v61, v63\n\t"
    "v_rcp_f32 v63, v63\n\t"
    "s_nop 1\n\t"
    "v_add_f32 v61, v61, v62\n\t"
    "v_mul_f32 v60, v60, v63\n\t"
    "v_min_u32 v61, -2, v61\n\t"
    "s_bitcmp1_b32 s25, 4\n\t"
    "s_cselect_b64 vcc, -1, 0\n\t"
    "s_waitcnt vmcnt(8)\n\t"
    TP(33)
    "ds_read_b64_tr_b16 v[194:195], v31 offset:0\n\t"
    "v_xor_b32 v58, 32, v31\n\t"
    "ds_read_b64_tr_b16 v[198:199], v58 offset:0\n\t"
    "v_xor_b32 v59, 64, v31\n\t"
    "ds_read_b64_tr_b16 v[202:203], v59 offset:0\n\t"
    "v_xor_b32 v58, 96, v31\n\t"
    "ds_read_b64_tr_b16 v[206:207], v58 offset:0\n\t"
    "v_xor_b32 v59, 128, v31\n\t"
    "ds_read_b64_tr_b16 v[210:211], v59 offset:0\n\t"
    "v_xor_b32 v58, 160, v31\n\t"
    "ds_read_b64_tr_b16 v[214:215], v58 offset:0\n\t"
    "v_xor_b32 v59, 192, v31\n\t"
    "ds_read_b64_tr_b16 v[218:219], v59 offset:0\n\t"
    "v_xor_b32 v58, 224, v31\n\t"
    "ds_read_b64_tr_b16 v[222:223], v58 offset:0\n\t"
    "s_waitcnt lgkmcnt(0)\n\t"
    "s_cbranch_vccz 96f\n\t"
    "v_lshrrev_b32 v59, 2, v29\n\t"
    "v_lshrrev_b32 v58, 4, v59\n\t"
    "v_lshlrev_b32 v57, 2, v58\n\t"
    "v_xor_b32 v57, v57, v59\n\t"
    "v_and_b32 v57, 15, v57\n\t"
    "v_lshlrev_b32 v57, 4, v57\n\t"
    "v_lshl_add_u32 v57, v58, 8, v57\n\t"
    "s_add_u32 m0, %[ldsw], 0\n\t"
    "s_add_u32 s90, s36, 0\n\t"
    "buffer_load_dwordx4 v57, s[64:67], s90 offen nt lds\n\t"
    "v_xor_b32 v58, 16, v57\n\t"
    "buffer_load_dwordx4 v58, s[64:67], s90 offen offset:1024 nt lds\n\t"
    "v_xor_b32 v58, 32, v57\n\t"
    "buffer_load_dwordx4 v58, s[64:67], s90 offen offset:2048 nt lds\n\t"
    "v_xor_b32 v58, 48, v57\n\t"
    "buffer_load_dwordx4 v58, s[64:67], s90 offen offset:3072 nt lds\n\t"
    "96:\n\t"
    "v_mfma_f32_16x16x32_bf16 v[0:3], v[192:195], v[16:19], v[0:3]\n\t"
    "v_mfma_f32_16x16x32_bf16 v[32:35], v[196:199], v[16:19], v[32:35]\n\t"
    "v_mfma_f32_16x16x32_bf16 v[36:39], v[200:203], v[16:19], v[36:39]\n\t"
    "v_mfma_f32_16x16x32_bf16 v[40:43], v[204:207], v[16:19], v[40:43]\n\t"
    "v_mfma_f32_16x16x32_bf16 v[44:47], v[208:211], v[16:19], v[44:47]\n\t"
    "v_mfma_f32_16x16x32_bf16 v[48:51], v[212:215], v[16:19], v[48:51]\n\t"
    "v_mfma_f32_16x16x32_bf16 v[52:55], v[216:219], v[16:19], v[52:55]\n\t"
    "v_mfma_f32_16x16x32_bf16 v[20:23], v[220:223], v[16:19], v[20:23]\n\t"
    "s_cbranch_vccnz 96f\n\t"
    "s_waitcnt vmcnt(4)\n\t"
    "96:\n\t"
    "s_waitcnt vmcnt(8)\n\t"
    TP(34)
    "ds_read_b64_tr_b16 v[224:225], v31 offset:4096\n\t"
    "v_xor_b32 v58, 32, v31\n\t"
    "ds_read_b64_tr_b16 v[228:229], v58 offset:4096\n\t"
    "v_xor_b32 v59, 64, v31\n\t"
    "ds_read_b64_tr_b16 v[232:233], v59 offset:4096\n\t"
    "v_xor_b32 v58, 96, v31\n\t"
    "ds_read_b64_tr_b16 v[236:237], v58 offset:4096\n\t"
    "v_xor_b32 v59, 128, v31\n\t"
    "ds_read_b64_tr_b16 v[240:241], v59 offset:4096\n\t"
    "v_xor_b32 v58, 160, v31\n\t"
    "ds_read_b64_tr_b16 v[244:245], v58 offset:4096\n\t"
    "v_xor_b32 v59, 192, v31\n\t"
    "ds_read_b64_tr_b16 v[248:249], v59 offset:4096\n\t"
    "v_xor_b32 v58, 224, v31\n\t"
    "ds_read_b64_tr_b16 v[252:253], v58 offset:4096\n\t"
    "s_waitcnt lgkmcnt(0)\n\t"
    "s_cbranch_vccz 96f\n\t"
    "s_add_u32 m0, %[ldsw], 4096\n\t"
    "s_add_u32 s90, s36, 16384\n\t"
    "buffer_load_dwordx4 v57, s[64:67], s90 offen nt lds\n\t"
    "v_xor_b32 v58, 16, v57\n\t"
    "buffer_load_dwordx4 v58, s[64:67], s90 offen offset:1024 nt lds\n\t"
    "v_xor_b32 v58, 32, v57\n\t"
    "buffer_load_dwordx4 v58, s[64:67], s90 offen offset:2048 nt lds\n\t"
    "v_xor_b32 v58, 48, v57\n\t"
    "buffer_load_dwordx4 v58, s[64:67], s90 offen offset:3072 nt lds\n\t"
    "96:\n\t"
    "s_cbranch_vccnz 96f\n\t"
    "s_waitcnt vmcnt(0)\n\t"
    "96:\n\t"
    "s_waitcnt vmcnt(8)\n\t"
    TP(35)
    "ds_read_b64_tr_b16 v[226:227], v31 offset:8192\n\t"
    "v_xor_b32 v58, 32, v31\n\t"
    "ds_read_b64_tr_b16 v[230:231], v58 offset:8192\n\t"
    "v_xor_b32 v59, 64, v31\n\t"
    "ds_read_b64_tr_b16 v[234:235], v59 offset:8192\n\t"
    "v_xor_b32 v58, 96, v31\n\t"
    "ds_read_b64_tr_b16 v[238:239], v58 offset:8192\n\t"
    "v_xor_b32 v59, 128, v31\n\t"
    "ds_read_b64_tr_b16 v[242:243], v59 offset:8192\n\t"
    "v_xor_b32 v58, 160, v31\n\t"
    "ds_read_b64_tr_b16 v[246:247], v58 offset:8192\n\t"
    "v_xor_b32 v59, 192, v31\n\t"
    "ds_read_b64_tr_b16 v[250:251], v59 offset:8192\n\t"
    "v_xor_b32 v58, 224, v31\n\t"
    "ds_read_b64_tr_b16 v[254:255], v58 offset:8192\n\t"
    "s_waitcnt lgkmcnt(7)\n\t"
    "v_mfma_f32_16x16x32_bf16 v[0:3], v[224:227], v[24:27], v[0:3]\n\t"
    "s_waitcnt lgkmcnt(6)\n\t"
    "v_mfma_f32_16x16x32_bf16 v[32:35], v[228:231], v[24:27], v[32:35]\n\t"
    "s_waitcnt lgkmcnt(5)\n\t"
    "v_mfma_f32_16x16x32_bf16 v[36:39], v[232:235], v[24:27], v[36:39]\n\t"
    "s_waitcnt lgkmcnt(4)\n\t"
    "v_mfma_f32_16x16x32_bf16 v[40:43], v[236:239], v[24:27], v[40:43]\n\t"
    "s_waitcnt lgkmcnt(3)\n\t"
    "v_mfma_f32_16x16x32_bf16 v[44:47], v[240:243], v[24:27], v[44:47]\n\t"
    "s_waitcnt lgkmcnt(2)\n\t"
    "v_mfma_f32_16x16x32_bf16 v[48:51], v[244:247], v[24:27], v[48:51]\n\t"
    "v_mov_b32_dpp v0, v32 row_shr:4 row_mask:0xf bank_mask:0x2\n\t"
    "v_mov_b32_dpp v1, v33 row_shr:4 row_mask:0xf bank_mask:0x2\n\t"
    "s_waitcnt lgkmcnt(1)\n\t"
    "v_mfma_f32_16x16x32_bf16 v[52:55], v[248:251], v[24:27], v[52:55]\n\t"
    "v_mov_b32_dpp v2, v34 row_shr:4 row_mask:0xf bank_mask:0x2\n\t"
    "v_mov_b32_dpp v3, v35 row_shr:4 row_mask:0xf bank_mask:0x2\n\t"
    "v_mov_b32_dpp v0, v36 row_shr:8 row_mask:0xf bank_mask:0x4\n\t"
    "s_waitcnt lgkmcnt(0)\n\t"
    "v_mfma_f32_16x16x32_bf16 v[20:23], v[252:255], v[24:27], v[20:23]\n\t"
    "v_mov_b32_dpp v1, v37 row_shr:8 row_mask:0xf bank_mask:0x4\n\t"
    "v_mov_b32_dpp v2, v38 row_shr:8 row_mask:0xf bank_mask:0x4\n\t"
    "v_mov_b32_dpp v3, v39 row_shr:8 row_mask:0xf bank_mask:0x4\n\t"
    "v_mov_b32_dpp v0, v40 row_shr:12 row_mask:0xf bank_mask:0x8\n\t"
    "v_mov_b32_dpp v1, v41 row_shr:12 row_mask:0xf bank_mask:0x8\n\t"
    "v_mov_b32_dpp v2, v42 row_shr:12 row_mask:0xf bank_mask:0x8\n\t"
    "v_mov_b32_dpp v3, v43 row_shr:12 row_mask:0xf bank_mask:0x8\n\t"
    "ds_write_b128 v4, v[0:3]\n\t"
    "v_mov_b32_dpp v44, v48 row_shr:4 row_mask:0xf bank_mask:0x2\n\t"
    "v_mov_b32_dpp v45, v49 row_shr:4 row_mask:0xf bank_mask:0x2\n\t"
    "v_mov_b32_dpp v46, v50 row_shr:4 row_mask:0xf bank_mask:0x2\n\t"
    "v_mov_b32_dpp v47, v51 row_shr:4 row_mask:0xf bank_mask:0x2\n\t"
    "v_mov_b32_dpp v44, v52 row_shr:8 row_mask:0xf bank_mask:0x4\n\t"
    "v_mov_b32_dpp v45, v53 row_shr:8 row_mask:0xf bank_mask:0x4\n\t"
    "v_mov_b32_dpp v46, v54 row_shr:8 row_mask:0xf bank_mask:0x4\n\t"
    "v_mov_b32_dpp v47, v55 row_shr:8 row_mask:0xf bank_mask:0x4\n\t"
    "v_mov_b32_dpp v44, v20 row_shr:12 row_mask:0xf bank_mask:0x8\n\t"
    "v_mov_b32_dpp v45, v21 row_shr:12 row_mask:0xf bank_mask:0x8\n\t"
    "v_mov_b32_dpp v46, v22 row_shr:12 row_mask:0xf bank_mask:0x8\n\t"
    "v_mov_b32_dpp v47, v23 row_shr:12 row_mask:0xf bank_mask:0x8\n\t"
    "ds_write_b128 v4, v[44:47] offset:256\n\t"
    "s_waitcnt lgkmcnt(0)\n\t"
    TP(14)
    "s_barrier\n\t"
    TP(15)
    "ds_read_b64 v[32:33], v28 offset:0\n\t"
    "ds_read_b64 v[34:35], v28 offset:2112\n\t"
    "ds_read_b64 v[36:37], v28 offset:4224\n\t"
    "ds_read_b64 v[38:39], v28 offset:6336\n\t"
    "s_waitcnt lgkmcnt(3)\n\t"
    "v_mul_f32_dpp v40, v60, v32 quad_perm:[0,0,0,0] row_mask:0xf bank_mask:0xf\n\t"
    "v_mul_f32_dpp v41, v60, v33 quad_perm:[0,0,0,0] row_mask:0xf bank_mask:0xf\n\t"
    "s_waitcnt lgkmcnt(2)\n\t"
    "v_fmac_f32_dpp v40, v60, v34 quad_perm:[1,1,1,1] row_mask:0xf bank_mask:0xf\n\t"
    "v_fmac_f32_dpp v41, v60, v35 quad_perm:[1,1,1,1] row_mask:0xf bank_mask:0xf\n\t"
    "s_waitcnt lgkmcnt(1)\n\t"
    "v_fmac_f32_dpp v40, v60, v36 quad_perm:[2,2,2,2] row_mask:0xf bank_mask:0xf\n\t"
    "v_fmac_f32_dpp v41, v60, v37 quad_perm:[2,2,2,2] row_mask:0xf bank_mask:0xf\n\t"
    "s_waitcnt lgkmcnt(0)\n\t"
    "v_fmac_f32_dpp v40, v60, v38 quad_perm:[3,3,3,3] row_mask:0xf bank_mask:0xf\n\t"
    "v_fmac_f32_dpp v41, v60, v39 quad_perm:[3,3,3,3] row_mask:0xf bank_mask:0xf\n\t"
    "v_cvt_pk_bf16_f32 v60, v40, v41\n\t"
    "v_min_u32 v60, -2, v60\n\t"
    "s_bitcmp1_b32 s25, 5\n\t"
    "s_cbranch_scc0 96f\n\t"
    "s_add_u32 m0, %[ldsw], 8192\n\t"
    "s_add_u32 s90, s36, 32768\n\t"
    "buffer_load_dwordx4 v57, s[64:67], s90 offen nt lds\n\t"
    "v_xor_b32 v58, 16, v57\n\t"
    "buffer_load_dwordx4 v58, s[64:67], s90 offen offset:1024 nt lds\n\t"
    "v_xor_b32 v58, 32, v57\n\t"
    "buffer_load_dwordx4 v58, s[64:67], s90 offen offset:2048 nt lds\n\t"
    "v_xor_b32 v58, 48, v57\n\t"
    "buffer_load_dwordx4 v58, s[64:67], s90 offen offset:3072 nt lds\n\t"
    "96:\n\t"
    "s_memrealtime s[86:87]\n\t"
    "v_lshlrev_b32 v0, 2, v29\n\t"
    "v_mov_b32 v8, -1\n\t"
    "v_mov_b32 v9, -1\n\t"
    "v_mov_b32 v10, -1\n\t"
    "v_mov_b32 v11, -1\n\t"
    "s_waitcnt lgkmcnt(0)\n\t"
    TP(16)
    STAMP2_STR
    "s_mov_b32 s88, s38\n\t"
    "77:\n\t"
    "s_sub_u32 s89, s86, s88\n\t"
    "s_cmp_le_i32 s89, " STR1(SLACK) "\n\t"
    "s_cbranch_scc1 76f\n\t"
    "s_add_u32 s88, s88, " STR1(GTICK) "\n\t"
    "s_bitset1_b32 s75, 31\n\t"
    "s_branch 77b\n\t"
    "76:\n\t"
    "s_memrealtime s[86:87]\n\t"
    "s_waitcnt lgkmcnt(0)\n\t"
    "s_sub_u32 s86, s86, s88\n\t"
    "s_cmp_lt_i32 s86, 0\n\t"
    "s_cbranch_scc1 76b\n\t"
    "buffer_store_dword v60, v29, %[rws], s31 offen sc1\n\t"
    "buffer_store_dword v61, v30, %[rws], s32 offen sc1\n\t"
    "s_memtime s[78:79]\n\t"
    "buffer_store_dword v12, v29, %[rws], s33 offen sc1\n\t"
    "buffer_store_dword v13, v30, %[rws], s34 offen sc1\n\t"
    "s_bitcmp1_b32 %[ldsw], 12\n\t"
    "s_cselect_b32 exec_hi, 0xffff, -1\n\t"
    "buffer_store_dwordx4 v[8:11], v0, %[rws], s37 offen sc1\n\t"
    "s_mov_b64 exec, -1\n\t"
    "s_nop 0\n\t"
    "s_branch 99f\n\t"
    "74:\n\t"
    "s_add_u32 s92, s92, 1\n\t"
    "s_cmp_gt_u32 s92, 16384\n\t"
    "s_cbranch_scc1 73b\n\t"
    "s_sleep 1\n\t"
    "s_branch 75f\n\t"
    "75:\n\t"
    "buffer_load_dwordx4 v[8:11], v51, %[rws], s30 offen sc1\n\t"
    "s_waitcnt vmcnt(0)\n\t"
    "s_branch 72b\n\t"
    "99:\n\t"
    XSTOP_STR
    ST_STR(3)
    TP(0)
    "v_mbcnt_lo_u32_b32 v24, -1, 0\n\t"
    "v_mbcnt_hi_u32_b32 v24, -1, v24\n\t"
    "v_and_b32 v21, 15, v24\n\t"
    "v_mul_u32_u24 v21, 14592, v21\n\t"
    "s_mov_b32 s92, 0\n\t"
    "s_cmp_lt_u32 s24, 4\n\t"
    "s_cbranch_scc1 29f\n\t"
    "s_add_u32 m0, s27, 0\n\t"
    "s_mov_b64 exec, 0xffff\n\t"
    "buffer_load_dword v21, %[rws], s43 offen sc1 lds\n\t"
    "s_mov_b64 exec, -1\n\t"
    "v_lshrrev_b32 v23, 4, v24\n\t"
    "v_lshlrev_b32 v20, 2, v23\n\t"
    "v_xor_b32 v20, v20, v24\n\t"
    "v_and_b32 v20, 15, v20\n\t"
    "v_lshlrev_b32 v20, 4, v20\n\t"
    "v_mul_u32_u24 v23, 14592, v23\n\t"
    "v_add_u32 v20, v20, v23\n\t"
    "v_lshrrev_b32 v23, 4, v24\n\t"
    "v_lshlrev_b32 v22, 11, v23\n\t"
    "v_bfe_u32 v23, v24, 2, 2\n\t"
    "v_lshl_add_u32 v22, v23, 6, v22\n\t"
    "v_and_b32 v23, 3, v24\n\t"
    "v_lshl_add_u32 v22, v23, 3, v22\n\t"
    "v_add_u32 v22, %[ldsw], v22\n\t"
    "95:\n\t"
    "s_memtime s[90:91]\n\t"
    "s_waitcnt lgkmcnt(0)\n\t"
    "s_sub_u32 s90, s90, s78\n\t"
    "s_cmp_lt_u32 s90, s74\n\t"
    "s_cbranch_scc1 95b\n\t"
    TP(1)
    "s_memrealtime s[82:83]\n\t"
    "s_add_u32 m0, s80, 0\n\t"
    "s_add_u32 s93, s42, 0\n\t"
    "buffer_load_dwordx4 v20, %[rws], s93 offen sc1 lds\n\t"
    "s_add_u32 m0, s80, 1024\n\t"
    "s_add_u32 s93, s42, 58368\n\t"
    "v_xor_b32 v23, 16, v20\n\t"
    "buffer_load_dwordx4 v23, %[rws], s93 offen sc1 lds\n\t"
    "s_add_u32 m0, s80, 2048\n\t"
    "s_add_u32 s93, s42, 116736\n\t"
    "v_xor_b32 v23, 32, v20\n\t"
    "buffer_load_dwordx4 v23, %[rws], s93 offen sc1 lds\n\t"
    "s_add_u32 m0, s80, 3072\n\t"
    "s_add_u32 s93, s42, 175104\n\t"
    "v_xor_b32 v23, 48, v20\n\t"
    "buffer_load_dwordx4 v23, %[rws], s93 offen sc1 lds\n\t"
    "ds_read_b64_tr_b16 v[32:33], v22 offset:12288\n\t"
    "ds_read_b64_tr_b16 v[34:35], v22 offset:12544\n\t"
    "ds_read_b64_tr_b16 v[36:37], v22 offset:12800\n\t"
    "ds_read_b64_tr_b16 v[38:39], v22 offset:13056\n\t"
    "ds_read_b64_tr_b16 v[40:41], v22 offset:13312\n\t"
    "ds_read_b64_tr_b16 v[42:43], v22 offset:13568\n\t"
    "ds_read_b64_tr_b16 v[44:45], v22 offset:13824\n\t"
    "ds_read_b64_tr_b16 v[46:47], v22 offset:14080\n\t"
    "ds_read_b64_tr_b16 v[48:49], v22 offset:12320\n\t"
    "ds_read_b64_tr_b16 v[50:51], v22 offset:12576\n\t"
    "ds_read_b64_tr_b16 v[52:53], v22 offset:12832\n\t"
    "ds_read_b64_tr_b16 v[54:55], v22 offset:13088\n\t"
    "ds_read_b64_tr_b16 v[56:57], v22 offset:13344\n\t"
    "ds_read_b64_tr_b16 v[58:59], v22 offset:13600\n\t"
    "ds_read_b64_tr_b16 v[60:61], v22 offset:13856\n\t"
    "ds_read_b64_tr_b16 v[62:63], v22 offset:14112\n\t"
    "v_lshrrev_b32 v23, 4, v24\n\t"
    "v_xor_b32 v23, v23, v24\n\t"
    "v_and_b32 v23, 3, v23\n\t"
    "v_lshlrev_b32 v22, 6, v23\n\t"
    "v_bfe_u32 v23, v24, 2, 2\n\t"
    "v_lshl_add_u32 v22, v23, 4, v22\n\t"
    "v_and_b32 v23, 15, v24\n\t"
    "v_lshl_add_u32 v22, v23, 8, v22\n\t"
    "v_add_u32 v22, s80, v22\n\t"
    "2:\n\t"
    "s_waitcnt vmcnt(4)\n\t"
    "v_mbcnt_lo_u32_b32 v0, -1, 0\n\t"
    "v_mbcnt_hi_u32_b32 v0, -1, v0\n\t"
    "v_and_b32 v0, 48, v0\n\t"
    "v_add_u32 v0, s27, v0\n\t"
    "ds_read_b128 v[16:19], v0 offset:0\n\t"
    "s_waitcnt lgkmcnt(0)\n\t"
    "v_max3_u32 v23, v16, v17, v18\n\t"
    "v_max_u32 v23, v23, v19\n\t"
    "v_cmp_eq_u32 vcc, -1, v23\n\t"
    "v_max3_f32 v23, v16, v17, v18\n\t"
    "s_cbranch_vccnz 21f\n\t"
    "v_max_f32 v23, v23, v19\n\t"
    "v_mov_b32 v0, v23\n\t"
    "s_nop 1\n\t"
    "v_permlane16_swap_b32 v23, v0\n\t"
    "v_max_f32 v23, v23, v0\n\t"
    "v_sub_f32 v16, v16, v23\n\t"
    "v_sub_f32 v17, v17, v23\n\t"
    "v_sub_f32 v18, v18, v23\n\t"
    "v_sub_f32 v19, v19, v23\n\t"
    "v_exp_f32 v16, v16\n\t"
    "v_exp_f32 v17, v17\n\t"
    "v_exp_f32 v18, v18\n\t"
    "v_exp_f32 v19, v19\n\t"
    "v_add_f32 v23, v16, v17\n\t"
    "v_add_f32 v0, v18, v19\n\t"
    "v_add_f32 v23, v23, v0\n\t"
    "v_mov_b32 v0, v23\n\t"
    "s_nop 1\n\t"
    "v_permlane16_swap_b32 v23, v0\n\t"
    "v_add_f32 v23, v23, v0\n\t"
    "v_rcp_f32 v23, v23\n\t"
    "s_nop 0\n\t"
    "v_mul_f32 v16, v16, v23\n\t"
    "v_mul_f32 v17, v17, v23\n\t"
    "v_mul_f32 v18, v18, v23\n\t"
    "v_mul_f32 v19, v19, v23\n\t"
    "v_xor_b32 v25, 16, v22\n\t"
    "v_xor_b32 v26, 32, v22\n\t"
    "v_xor_b32 v27, 48, v22\n\t"
    "s_waitcnt vmcnt(0)\n\t"
    TP(2)
    "ds_read_b128 v[0:3], v22 offset:0\n\t"
    "ds_read_b128 v[4:7], v25 offset:0\n\t"
    "ds_read_b128 v[8:11], v26 offset:0\n\t"
    "ds_read_b128 v[12:15], v27 offset:0\n\t"
    "s_waitcnt lgkmcnt(3)\n\t"
    "v_mfma_f32_16x16x32_bf16 v[24:27], v[0:3], v[32:35], 0\n\t"
    "v_max3_u32 v23, v0, v1, v2\n\t"
    "v_mfma_f32_16x16x32_bf16 v[28:31], v[0:3], v[48:51], 0\n\t"
    "s_waitcnt lgkmcnt(2)\n\t"
    "v_mfma_f32_16x16x32_bf16 v[24:27], v[4:7], v[36:39], v[24:27]\n\t"
    "v_max3_u32 v23, v23, v3, v4\n\t"
    "v_mfma_f32_16x16x32_bf16 v[28:31], v[4:7], v[52:55], v[28:31]\n\t"
    "v_max3_u32 v23, v23, v5, v6\n\t"
    "s_waitcnt lgkmcnt(1)\n\t"
    "v_mfma_f32_16x16x32_bf16 v[24:27], v[8:11], v[40:43], v[24:27]\n\t"
    "v_max3_u32 v23, v23, v7, v8\n\t"
    "v_mfma_f32_16x16x32_bf16 v[28:31], v[8:11], v[56:59], v[28:31]\n\t"
    "v_max3_u32 v23, v23, v9, v10\n\t"
    "s_waitcnt lgkmcnt(0)\n\t"
    "v_mfma_f32_16x16x32_bf16 v[24:27], v[12:15], v[44:47], v[24:27]\n\t"
    "v_max3_u32 v23, v23, v11, v12\n\t"
    "v_mfma_f32_16x16x32_bf16 v[28:31], v[12:15], v[60:63], v[28:31]\n\t"
    "v_max3_u32 v23, v23, v13, v14\n\t"
    "v_max_u32 v23, v23, v15\n\t"
    "v_cmp_eq_u32 vcc, -1, v23\n\t"
    "v_mbcnt_lo_u32_b32 v0, -1, 0\n\t"
    "v_mbcnt_hi_u32_b32 v0, -1, v0\n\t"
    "s_cbranch_vccnz 21f\n\t"
    "3:\n\t"
    "v_lshlrev_b32 v0, 2, v0\n\t"
    "v_add_u32 v1, s45, v0\n\t"
    "v_mul_f32 v24, v24, v16\n\t"
    "v_mul_f32 v28, v28, v16\n\t"
    "v_fmac_f32 v24, v25, v17\n\t"
    "v_fmac_f32 v28, v29, v17\n\t"
    "v_fmac_f32 v24, v26, v18\n\t"
    "v_fmac_f32 v28, v30, v18\n\t"
    "v_fmac_f32 v24, v27, v19\n\t"
    "v_fmac_f32 v28, v31, v19\n\t"
    "v_lshl_add_u32 v2, v0, 1, s77\n\t"
    "s_nop 0\n\t"
    "v_permlane16_swap_b32 v24, v28\n\t"
    "v_add_f32 v24, v24, v28\n\t"
    "ds_write_b32 v1, v24 offset:0\n\t"
    "6:\n\t"
    "s_waitcnt lgkmcnt(0)\n\t"
    TP(3)
    "s_barrier\n\t"
    TP(4)
    "s_memtime s[78:79]\n\t"
    "s_cmp_lg_u32 s77, s45\n\t"
    "s_cbranch_scc1 9f\n\t"
    "ds_read_b64 v[4:5], v2 offset:0\n\t"
    "ds_read_b64 v[6:7], v2 offset:512\n\t"
    "ds_read_b64 v[8:9], v2 offset:1024\n\t"
    "ds_read_b64 v[10:11], v2 offset:1536\n\t"
    "s_cmp_lg_u32 s40, 0\n\t"
    "s_cselect_b32 exec_hi, -1, 0\n\t"
    "v_subrev_u32 v3, s40, v0\n\t"
    "v_subrev_u32 v3, 128, v3\n\t"
    "s_mov_b32 vcc_lo, 0\n\t"
    "s_mov_b32 vcc_hi, -1\n\t"
    "v_cndmask_b32 v0, v0, v3, vcc\n\t"
    "v_add_u32 v0, s44, v0\n\t"
    "s_waitcnt lgkmcnt(2)\n\t"
    "v_pk_add_f32 v[4:5], v[4:5], v[6:7]\n\t"
    "s_waitcnt lgkmcnt(0)\n\t"
    "v_pk_add_f32 v[8:9], v[8:9], v[10:11]\n\t"
    "v_pk_add_f32 v[4:5], v[4:5], v[8:9]\n\t"
    "v_cvt_pk_bf16_f32 v4, v4, v5\n\t"
    "v_min_u32 v4, -2, v4\n\t"
    "buffer_store_dword v4, v0, %[rws], 0 offen sc1\n\t"
    "s_mov_b64 exec, -1\n\t"
    TP(5)
    "s_branch 9f\n\t"
    "21:\n\t"
    "s_waitcnt vmcnt(0)\n\t"
    "23:\n\t"
    "s_add_u32 s92, s92, 1\n\t"
    "s_cmp_gt_u32 s92, 16384\n\t"
    "s_cbranch_scc1 24f\n\t"
    "s_waitcnt lgkmcnt(0)\n\t"
    "s_add_u32 s82, s82, " STR1(GTICK) "\n\t"
    "97:\n\t"
    "s_memrealtime s[90:91]\n\t"
    "s_waitcnt lgkmcnt(0)\n\t"
    "s_sub_u32 s90, s90, s82\n\t"
    "s_cmp_lt_i32 s90, 0\n\t"
    "s_cbranch_scc1 97b\n\t"
    "s_branch 25f\n\t"
    "24:\n\t"
    "v_mbcnt_lo_u32_b32 v0, -1, 0\n\t"
    "v_mbcnt_hi_u32_b32 v0, -1, v0\n\t"
    "s_branch 3b\n\t"
    "25:\n\t"
    "s_bitset1_b32 s75, 31\n\t"
    "s_add_u32 m0, s27, 0\n\t"
    "s_mov_b64 exec, 0xffff\n\t"
    "buffer_load_dword v21, %[rws], s43 offen sc1 lds\n\t"
    "s_mov_b64 exec, -1\n\t"
    "s_add_u32 m0, s80, 0\n\t"
    "s_add_u32 s93, s42, 0\n\t"
    "buffer_load_dwordx4 v20, %[rws], s93 offen sc1 lds\n\t"
    "s_add_u32 m0, s80, 1024\n\t"
    "s_add_u32 s93, s42, 58368\n\t"
    "v_xor_b32 v23, 16, v20\n\t"
    "buffer_load_dwordx4 v23, %[rws], s93 offen sc1 lds\n\t"
    "s_add_u32 m0, s80, 2048\n\t"
    "s_add_u32 s93, s42, 116736\n\t"
    "v_xor_b32 v23, 32, v20\n\t"
    "buffer_load_dwordx4 v23, %[rws], s93 offen sc1 lds\n\t"
    "s_add_u32 m0, s80, 3072\n\t"
    "s_add_u32 s93, s42, 175104\n\t"
    "v_xor_b32 v23, 48, v20\n\t"
    "buffer_load_dwordx4 v23, %[rws], s93 offen sc1 lds\n\t"
    "s_branch 2b\n\t"
    "29:\n\t"
    "v_lshrrev_b32 v23, 4, v24\n\t"
    "v_lshlrev_b32 v22, 11, v23\n\t"
    "v_bfe_u32 v23, v24, 2, 2\n\t"
    "v_lshl_add_u32 v22, v23, 6, v22\n\t"
    "v_and_b32 v23, 3, v24\n\t"
    "v_lshl_add_u32 v22, v23, 3, v22\n\t"
    "v_add_u32 v22, %[ldsw], v22\n\t"
    "ds_read_b64_tr_b16 v[32:33], v22 offset:12288\n\t"
    "ds_read_b64_tr_b16 v[34:35], v22 offset:12544\n\t"
    "ds_read_b64_tr_b16 v[36:37], v22 offset:12800\n\t"
    "ds_read_b64_tr_b16 v[38:39], v22 offset:13056\n\t"
    "ds_read_b64_tr_b16 v[40:41], v22 offset:13312\n\t"
    "ds_read_b64_tr_b16 v[42:43], v22 offset:13568\n\t"
    "ds_read_b64_tr_b16 v[44:45], v22 offset:13824\n\t"
    "ds_read_b64_tr_b16 v[46:47], v22 offset:14080\n\t"
    "ds_read_b64_tr_b16 v[48:49], v22 offset:12320\n\t"
    "ds_read_b64_tr_b16 v[50:51], v22 offset:12576\n\t"
    "ds_read_b64_tr_b16 v[52:53], v22 offset:12832\n\t"
    "ds_read_b64_tr_b16 v[54:55], v22 offset:13088\n\t"
    "ds_read_b64_tr_b16 v[56:57], v22 offset:13344\n\t"
    "ds_read_b64_tr_b16 v[58:59], v22 offset:13600\n\t"
    "ds_read_b64_tr_b16 v[60:61], v22 offset:13856\n\t"
    "ds_read_b64_tr_b16 v[62:63], v22 offset:14112\n\t"
    "v_lshrrev_b32 v23, 4, v24\n\t"
    "v_lshlrev_b32 v20, 2, v23\n\t"
    "v_xor_b32 v20, v20, v24\n\t"
    "v_and_b32 v20, 15, v20\n\t"
    "v_lshlrev_b32 v20, 4, v20\n\t"
    "v_mul_u32_u24 v23, 14592, v23\n\t"
    "v_add_u32 v20, v20, v23\n\t"
    "v_lshrrev_b32 v23, 4, v24\n\t"
    "v_xor_b32 v23, v23, v24\n\t"
    "v_and_b32 v23, 3, v23\n\t"
    "v_lshlrev_b32 v22, 6, v23\n\t"
    "v_bfe_u32 v23, v24, 2, 2\n\t"
    "v_lshl_add_u32 v22, v23, 4, v22\n\t"
    "v_and_b32 v23, 15, v24\n\t"
    "v_lshl_add_u32 v22, v23, 8, v22\n\t"
    "v_add_u32 v22, s80, v22\n\t"
    "95:\n\t"
    "s_memtime s[90:91]\n\t"
    "s_waitcnt lgkmcnt(0)\n\t"
    "s_sub_u32 s90, s90, s78\n\t"
    "s_cmp_lt_u32 s90, s74\n\t"
    "s_cbranch_scc1 95b\n\t"
    TP(1)
    "s_memrealtime s[82:83]\n\t"
    "s_sub_u32 s94, s43, s40\n\t"
    "s_sub_u32 s95, s42, s40\n\t"
    "31:\n\t"
    "s_cmp_lg_u32 s40, 0\n\t"
    "s_cbranch_scc0 32f\n\t"
    "s_add_u32 m0, s27, 64\n\t"
    "s_mov_b64 exec, 0xffff\n\t"
    "buffer_load_dword v21, %[rws], s94 offen sc1 lds\n\t"
    "s_mov_b64 exec, -1\n\t"
    "s_add_u32 m0, s80, 4096\n\t"
    "s_add_u32 s93, s95, 0\n\t"
    "buffer_load_dwordx4 v20, %[rws], s93 offen sc1 lds\n\t"
    "s_add_u32 m0, s80, 5120\n\t"
    "s_add_u32 s93, s95, 58368\n\t"
    "v_xor_b32 v23, 16, v20\n\t"
    "buffer_load_dwordx4 v23, %[rws], s93 offen sc1 lds\n\t"
    "s_add_u32 m0, s80, 6144\n\t"
    "s_add_u32 s93, s95, 116736\n\t"
    "v_xor_b32 v23, 32, v20\n\t"
    "buffer_load_dwordx4 v23, %[rws], s93 offen sc1 lds\n\t"
    "s_add_u32 m0, s80, 7168\n\t"
    "s_add_u32 s93, s95, 175104\n\t"
    "v_xor_b32 v23, 48, v20\n\t"
    "buffer_load_dwordx4 v23, %[rws], s93 offen sc1 lds\n\t"
    "32:\n\t"
    "s_add_u32 m0, s27, 0\n\t"
    "s_mov_b64 exec, 0xffff\n\t"
    "buffer_load_dword v21, %[rws], s43 offen sc1 lds\n\t"
    "s_mov_b64 exec, -1\n\t"
    "s_add_u32 m0, s80, 0\n\t"
    "s_add_u32 s93, s42, 0\n\t"
    "buffer_load_dwordx4 v20, %[rws], s93 offen sc1 lds\n\t"
    "s_add_u32 m0, s80, 1024\n\t"
    "s_add_u32 s93, s42, 58368\n\t"
    "v_xor_b32 v23, 16, v20\n\t"
    "buffer_load_dwordx4 v23, %[rws], s93 offen sc1 lds\n\t"
    "s_add_u32 m0, s80, 2048\n\t"
    "s_add_u32 s93, s42, 116736\n\t"
    "v_xor_b32 v23, 32, v20\n\t"
    "buffer_load_dwordx4 v23, %[rws], s93 offen sc1 lds\n\t"
    "s_add_u32 m0, s80, 3072\n\t"
    "s_add_u32 s93, s42, 175104\n\t"
    "v_xor_b32 v23, 48, v20\n\t"
    "buffer_load_dwordx4 v23, %[rws], s93 offen sc1 lds\n\t"
    "s_cmp_gt_u32 s81, 5\n\t"
    "s_cbranch_scc1 92f\n\t"
    "s_and_b32 s86, s81, 1\n\t"
    "s_lshl_b32 s86, s86, 6\n\t"
    "s_bitcmp1_b32 %[outw], 11\n\t"
    "s_cselect_b32 s87, 96, 0\n\t"
    "s_lshl4_add_u32 s87, s81, s87\n\t"
    "s_load_dwordx2 s[52:53], %[ka], s86 offset:0\n\t"
    "s_load_dwordx2 s[56:57], %[ka], s86 offset:8\n\t"
    "s_load_dwordx2 s[60:61], %[ka], s86 offset:16\n\t"
    "s_load_dwordx2 s[64:65], %[ka], s86 offset:32\n\t"
    "s_load_dwordx2 s[72:73], %[ka], s87 offset:160\n\t"
    "s_mul_i32 s86, s81, 6\n\t"
    "s_mov_b32 s88, 0xc23d6d74\n\t"
    "s_lshr_b32 s25, s88, s86\n\t"
    "s_lshr_b32 s86, s81, 2\n\t"
    "s_lshl1_add_u32 s86, s86, s81\n\t"
    "s_mul_i32 s86, s86, 1792\n\t"
    "s_add_u32 s86, s86, %[qb]\n\t"
    "s_add_u32 s29, s86, -128\n\t"
    "s_add_u32 s87, s81, 2\n\t"
    "s_lshr_b32 s87, s87, 2\n\t"
    "s_lshl_b32 s86, s87, 25\n\t"
    "s_add_u32 s35, %[kv0], s86\n\t"
    "s_lshl1_add_u32 s87, s87, s81\n\t"
    "s_mul_i32 s87, s87, 1792\n\t"
    "s_add_u32 s86, s87, -128\n\t"
    "s_lshl_b32 s88, s81, 6\n\t"
    "s_sub_u32 s88, s88, 256\n\t"
    "s_and_b32 s89, s81, 5\n\t"
    "s_cselect_b32 s86, s86, s88\n\t"
    "s_add_u32 s30, s86, %[qb]\n\t"
    "s_add_u32 s87, s87, %[mine]\n\t"
    "s_lshl_b32 s86, %[w], 7\n\t"
    "s_lshl1_add_u32 s31, s86, s87\n\t"
    "s_add_u32 s87, s87, s86\n\t"
    "s_add_u32 s32, s87, 1024\n\t"
    "s_mov_b32 s86, -3584\n\t"
    "s_cmp_lt_u32 s81, 2\n\t"
    "s_cselect_b32 s86, 0x7f000000, s86\n\t"
    "s_add_u32 s33, s31, s86\n\t"
    "s_add_u32 s34, s32, s86\n\t"
    "s_min_u32 s86, s81, 1\n\t"
    "s_lshl_b32 s86, s86, 25\n\t"
    "s_add_u32 s36, %[kv0], s86\n\t"
    "s_lshl_b32 s86, s81, 3\n\t"
    "s_and_b32 s87, %[w], 2\n\t"
    "s_lshl1_add_u32 s86, s87, s86\n\t"
    "s_lshr_b32 s86, 0x765342, s86\n\t"
    "s_and_b32 s86, s86, 7\n\t"
    "s_mul_i32 s86, s86, 1792\n\t"
    "s_and_b32 s87, %[w], 1\n\t"
    "s_lshl_b32 s87, s87, 10\n\t"
    "s_add_u32 s86, s86, s87\n\t"
    "s_add_u32 s86, s86, %[mine]\n\t"
    "s_cmp_lt_u32 s81, 3\n\t"
    "s_cselect_b32 s37, s86, 0x7f000000\n\t"
    "92:\n\t"
    "v_lshlrev_b32 v0, 2, v24\n\t"
    "v_lshrrev_b32 v28, 2, v0\n\t"
    "v_and_b32 v28, 48, v28\n\t"
    "v_add_u32 v28, s27, v28\n\t"
    "v_xor_b32 v25, 16, v22\n\t"
    "v_xor_b32 v26, 32, v22\n\t"
    "v_xor_b32 v27, 48, v22\n\t"
    "s_cmp_lg_u32 s40, 0\n\t"
    "s_waitcnt lgkmcnt(0)\n\t"
    "s_cbranch_scc0 34f\n\t"
    "s_waitcnt vmcnt(5)\n\t"
    "ds_read_b128 v[16:19], v28 offset:64\n\t"
    "ds_read_b128 v[0:3], v22 offset:4096\n\t"
    "ds_read_b128 v[4:7], v25 offset:4096\n\t"
    "ds_read_b128 v[8:11], v26 offset:4096\n\t"
    "ds_read_b128 v[12:15], v27 offset:4096\n\t"
    "s_waitcnt lgkmcnt(4)\n\t"
    "v_max3_f32 v21, v16, v17, v18\n\t"
    "v_max3_u32 v23, v16, v17, v18\n\t"
    "v_max_f32 v21, v21, v19\n\t"
    "v_mov_b32 v20, v21\n\t"
    "s_waitcnt lgkmcnt(3)\n\t"
    "v_mfma_f32_16x16x32_bf16 v[24:27], v[0:3], v[32:35], 0\n\t"
    "v_max3_u32 v23, v23, v19, v0\n\t"
    "v_permlane16_swap_b32 v21, v20\n\t"
    "v_cmp_eq_u32 vcc, -1, v23\n\t"
    "v_mfma_f32_16x16x32_bf16 v[28:31], v[0:3], v[48:51], 0\n\t"
    "v_max_f32 v21, v21, v20\n\t"
    "v_max3_u32 v23, v23, v1, v2\n\t"
    "s_cbranch_vccnz 41f\n\t"
    "s_waitcnt lgkmcnt(2)\n\t"
    "v_mfma_f32_16x16x32_bf16 v[24:27], v[4:7], v[36:39], v[24:27]\n\t"
    "v_sub_f32 v16, v16, v21\n\t"
    "v_sub_f32 v17, v17, v21\n\t"
    "v_mfma_f32_16x16x32_bf16 v[28:31], v[4:7], v[52:55], v[28:31]\n\t"
    "v_sub_f32 v18, v18, v21\n\t"
    "v_sub_f32 v19, v19, v21\n\t"
    "s_waitcnt lgkmcnt(1)\n\t"
    "v_mfma_f32_16x16x32_bf16 v[24:27], v[8:11], v[40:43], v[24:27]\n\t"
    "v_exp_f32 v16, v16\n\t"
    "v_max3_u32 v23, v23, v3, v4\n\t"
    "v_mfma_f32_16x16x32_bf16 v[28:31], v[8:11], v[56:59], v[28:31]\n\t"
    "v_exp_f32 v17, v17\n\t"
    "v_max3_u32 v23, v23, v5, v6\n\t"
    "s_waitcnt lgkmcnt(0)\n\t"
    "v_mfma_f32_16x16x32_bf16 v[24:27], v[12:15], v[44:47], v[24:27]\n\t"
    "v_exp_f32 v18, v18\n\t"
    "v_max3_u32 v23, v23, v7, v8\n\t"
    "v_mfma_f32_16x16x32_bf16 v[28:31], v[12:15], v[60:63], v[28:31]\n\t"
    "v_exp_f32 v19, v19\n\t"
    "v_max3_u32 v23, v23, v9, v10\n\t"
    "v_add_f32 v21, v16, v17\n\t"
    "v_add_f32 v20, v18, v19\n\t"
    "v_add_f32 v21, v21, v20\n\t"
    "v_mov_b32 v20, v21\n\t"
    "v_max3_u32 v23, v23, v11, v12\n\t"
    "v_max3_u32 v23, v23, v13, v14\n\t"
    "v_permlane16_swap_b32 v21, v20\n\t"
    "v_max_u32 v23, v23, v15\n\t"
    "v_add_f32 v21, v21, v20\n\t"
    "v_cmp_eq_u32 vcc, -1, v23\n\t"
    "v_rcp_f32 v21, v21\n\t"
    "v_mbcnt_lo_u32_b32 v0, -1, 0\n\t"
    "v_mbcnt_hi_u32_b32 v0, -1, v0\n\t"
    "s_cbranch_vccnz 41f\n\t"
    "33:\n\t"
    "v_mul_f32 v24, v24, v16\n\t"
    "v_mul_f32 v28, v28, v16\n\t"
    "v_fmac_f32 v24, v25, v17\n\t"
    "v_fmac_f32 v28, v29, v17\n\t"
    "v_fmac_f32 v24, v26, v18\n\t"
    "v_fmac_f32 v28, v30, v18\n\t"
    "v_fmac_f32 v24, v27, v19\n\t"
    "v_fmac_f32 v28, v31, v19\n\t"
    "v_lshlrev_b32 v0, 2, v0\n\t"
    "v_add_u32 v1, s45, v0\n\t"
    "v_permlane16_swap_b32 v24, v28\n\t"
    "v_add_f32 v24, v24, v28\n\t"
    "v_mul_f32 v24, v24, v21\n\t"
    "ds_write_b32 v1, v24 offset:256\n\t"
    "v_lshrrev_b32 v28, 2, v0\n\t"
    "v_and_b32 v28, 48, v28\n\t"
    "v_add_u32 v28, s27, v28\n\t"
    "v_xor_b32 v25, 16, v22\n\t"
    "v_xor_b32 v26, 32, v22\n\t"
    "v_xor_b32 v27, 48, v22\n\t"
    "34:\n\t"
    "s_waitcnt vmcnt(0)\n\t"
    TP(2)
    "ds_read_b128 v[16:19], v28 offset:0\n\t"
    "ds_read_b128 v[0:3], v22 offset:0\n\t"
    "ds_read_b128 v[4:7], v25 offset:0\n\t"
    "ds_read_b128 v[8:11], v26 offset:0\n\t"
    "ds_read_b128 v[12:15], v27 offset:0\n\t"
    "s_waitcnt lgkmcnt(4)\n\t"
    "v_max3_f32 v21, v16, v17, v18\n\t"
    "v_max3_u32 v23, v16, v17, v18\n\t"
    "v_max_f32 v21, v21, v19\n\t"
    "v_mov_b32 v20, v21\n\t"
    "s_waitcnt lgkmcnt(3)\n\t"
    "v_mfma_f32_16x16x32_bf16 v[24:27], v[0:3], v[32:35], 0\n\t"
    "v_max3_u32 v23, v23, v19, v0\n\t"
    "v_permlane16_swap_b32 v21, v20\n\t"
    "v_cmp_eq_u32 vcc, -1, v23\n\t"
    "v_mfma_f32_16x16x32_bf16 v[28:31], v[0:3], v[48:51], 0\n\t"
    "v_max_f32 v21, v21, v20\n\t"
    "v_max3_u32 v23, v23, v1, v2\n\t"
    "s_cbranch_vccnz 41f\n\t"
    "s_waitcnt lgkmcnt(2)\n\t"
    "v_mfma_f32_16x16x32_bf16 v[24:27], v[4:7], v[36:39], v[24:27]\n\t"
    "v_sub_f32 v16, v16, v21\n\t"
    "v_sub_f32 v17, v17, v21\n\t"
    "v_mfma_f32_16x16x32_bf16 v[28:31], v[4:7], v[52:55], v[28:31]\n\t"
    "v_sub_f32 v18, v18, v21\n\t"
    "v_sub_f32 v19, v19, v21\n\t"
    "s_waitcnt lgkmcnt(1)\n\t"
    "v_mfma_f32_16x16x32_bf16 v[24:27], v[8:11], v[40:43], v[24:27]\n\t"
    "v_exp_f32 v16, v16\n\t"
    "v_max3_u32 v23, v23, v3, v4\n\t"
    "v_mfma_f32_16x16x32_bf16 v[28:31], v[8:11], v[56:59], v[28:31]\n\t"
    "v_exp_f32 v17, v17\n\t"
    "v_max3_u32 v23, v23, v5, v6\n\t"
    "s_waitcnt lgkmcnt(0)\n\t"
    "v_mfma_f32_16x16x32_bf16 v[24:27], v[12:15], v[44:47], v[24:27]\n\t"
    "v_exp_f32 v18, v18\n\t"
    "v_max3_u32 v23, v23, v7, v8\n\t"
    "v_mfma_f32_16x16x32_bf16 v[28:31], v[12:15], v[60:63], v[28:31]\n\t"
    "v_exp_f32 v19, v19\n\t"
    "v_max3_u32 v23, v23, v9, v10\n\t"
    "v_add_f32 v21, v16, v17\n\t"
    "v_add_f32 v20, v18, v19\n\t"
    "v_add_f32 v21, v21, v20\n\t"
    "v_mov_b32 v20, v21\n\t"
    "v_max3_u32 v23, v23, v11, v12\n\t"
    "v_max3_u32 v23, v23, v13, v14\n\t"
    "v_permlane16_swap_b32 v21, v20\n\t"
    "v_max_u32 v23, v23, v15\n\t"
    "v_add_f32 v21, v21, v20\n\t"
    "v_cmp_eq_u32 vcc, -1, v23\n\t"
    "v_rcp_f32 v21, v21\n\t"
    "v_mbcnt_lo_u32_b32 v0, -1, 0\n\t"
    "v_mbcnt_hi_u32_b32 v0, -1, v0\n\t"
    "s_cbranch_vccnz 41f\n\t"
    "35:\n\t"
    "v_mul_f32 v24, v24, v16\n\t"
    "v_mul_f32 v28, v28, v16\n\t"
    "v_fmac_f32 v24, v25, v17\n\t"
    "v_fmac_f32 v28, v29, v17\n\t"
    "v_fmac_f32 v24, v26, v18\n\t"
    "v_fmac_f32 v28, v30, v18\n\t"
    "v_fmac_f32 v24, v27, v19\n\t"
    "v_fmac_f32 v28, v31, v19\n\t"
    "v_lshlrev_b32 v0, 2, v0\n\t"
    "v_add_u32 v1, s45, v0\n\t"
    "v_permlane16_swap_b32 v24, v28\n\t"
    "v_add_f32 v24, v24, v28\n\t"
    "v_mul_f32 v24, v24, v21\n\t"
    "ds_write_b32 v1, v24 offset:0\n\t"
    "v_lshl_add_u32 v2, v0, 1, s77\n\t"
    "s_branch 6b\n\t"
    "41:\n\t"
    "s_waitcnt vmcnt(0) lgkmcnt(0)\n\t"
    "s_nop 7\n\t"
    "v_mbcnt_lo_u32_b32 v0, -1, 0\n\t"
    "v_mbcnt_hi_u32_b32 v0, -1, v0\n\t"
    "43:\n\t"
    "s_add_u32 s92, s92, 1\n\t"
    "s_cmp_gt_u32 s92, 16384\n\t"
    "s_cbranch_scc1 35b\n\t"
    "s_branch 45f\n\t"
    "45:\n\t"
    "v_mbcnt_lo_u32_b32 v24, -1, 0\n\t"
    "v_mbcnt_hi_u32_b32 v24, -1, v24\n\t"
    "v_and_b32 v21, 15, v24\n\t"
    "v_mul_u32_u24 v21, 14592, v21\n\t"
    "v_lshrrev_b32 v23, 4, v24\n\t"
    "v_lshlrev_b32 v20, 2, v23\n\t"
    "v_xor_b32 v20, v20, v24\n\t"
    "v_and_b32 v20, 15, v20\n\t"
    "v_lshlrev_b32 v20, 4, v20\n\t"
    "v_mul_u32_u24 v23, 14592, v23\n\t"
    "v_add_u32 v20, v20, v23\n\t"
    "s_bitset1_b32 s75, 31\n\t"
    "s_waitcnt lgkmcnt(0)\n\t"
    "s_add_u32 s82, s82, " STR1(GTICK) "\n\t"
    "97:\n\t"
    "s_memrealtime s[90:91]\n\t"
    "s_waitcnt lgkmcnt(0)\n\t"
    "s_sub_u32 s90, s90, s82\n\t"
    "s_cmp_lt_i32 s90, 0\n\t"
    "s_cbranch_scc1 97b\n\t"
    "s_branch 31b\n\t"
    "9:\n\t"
    ST_STR(4)
    "s_cmp_eq_u32 s24, 5\n\t"
    "s_cbranch_scc1 .Lgqa_last\n\t"
    ".Lgqa_s2:\n\t"
    TP(0)
    "s_mov_b32 s84, 0xff00ff00\n\t"
    "s_mov_b32 s85, 0xff00ff00\n\t"
    "s_bitcmp1_b32 s46, 2\n\t"
    "s_cbranch_scc0 1f\n\t"
    "v_mbcnt_lo_u32_b32 v1, -1, 0\n\t"
    "v_mbcnt_hi_u32_b32 v1, -1, v1\n\t"
    "v_lshrrev_b32 v0, 2, v1\n\t"
    "v_and_b32 v1, 3, v1\n\t"
    "v_lshlrev_b32 v1, 4, v1\n\t"
    "v_lshl_add_u32 v0, v0, 13, v1\n\t"
    "s_add_u32 m0, %[ldsw], 12288\n\t"
    "s_add_u32 s90, %[wq_off], 0\n\t"
    "buffer_load_dwordx4 v0, s[68:71], s90 offen lds\n\t"
    "s_add_u32 m0, %[ldsw], 13312\n\t"
    "s_add_u32 s90, %[wq_off], 131072\n\t"
    "buffer_load_dwordx4 v0, s[68:71], s90 offen lds\n\t"
    "s_add_u32 m0, %[ldsw], 14336\n\t"
    "s_add_u32 s90, %[wq_off], 262144\n\t"
    "buffer_load_dwordx4 v0, s[68:71], s90 offen lds\n\t"
    "s_add_u32 m0, %[ldsw], 15360\n\t"
    "s_add_u32 s90, %[wq_off], 393216\n\t"
    "buffer_load_dwordx4 v0, s[68:71], s90 offen lds\n\t"
    "s_add_u32 m0, %[ldsw], 16384\n\t"
    "s_add_u32 s90, %[wq_off], 524288\n\t"
    "buffer_load_dwordx4 v0, s[68:71], s90 offen lds\n\t"
    "s_add_u32 m0, %[ldsw], 17408\n\t"
    "s_add_u32 s90, %[wq_off], 655360\n\t"
    "buffer_load_dwordx4 v0, s[68:71], s90 offen lds\n\t"
    "s_add_u32 m0, %[ldsw], 18432\n\t"
    "s_add_u32 s90, %[wq_off], 786432\n\t"
    "buffer_load_dwordx4 v0, s[68:71], s90 offen lds\n\t"
    "s_add_u32 m0, %[ldsw], 19456\n\t"
    "s_add_u32 s90, %[wq_off], 917504\n\t"
    "buffer_load_dwordx4 v0, s[68:71], s90 offen lds\n\t"
    "1:\n\t"
    "v_mbcnt_lo_u32_b32 v4, -1, 0\n\t"
    "v_mbcnt_hi_u32_b32 v4, -1, v4\n\t"
    "v_lshrrev_b32 v5, 4, v4\n\t"
    "v_and_b32 v6, 7, v4\n\t"
    "v_lshl_add_u32 v5, v5, 3, v6\n\t"
    "v_lshl_add_u32 v5, v5, 4, s50\n\t"
    "ds_read_b128 v[0:3], v5\n\t"
    "s_waitcnt lgkmcnt(0)\n\t"
    "v_cndmask_b32_e64 v0, v0, 0, s[84:85]\n\t"
    "v_cndmask_b32_e64 v1, v1, 0, s[84:85]\n\t"
    "v_cndmask_b32_e64 v2, v2, 0, s[84:85]\n\t"
    "v_cndmask_b32_e64 v3, v3, 0, s[84:85]\n\t"
    "v_lshlrev_b32 v24, 16, v0\n\t"
    "v_and_b32 v25, 0xffff0000, v0\n\t"
    "v_lshlrev_b32 v26, 16, v1\n\t"
    "v_and_b32 v27, 0xffff0000, v1\n\t"
    "v_lshlrev_b32 v28, 16, v2\n\t"
    "v_and_b32 v29, 0xffff0000, v2\n\t"
    "v_lshlrev_b32 v30, 16, v3\n\t"
    "v_and_b32 v31, 0xffff0000, v3\n\t"
    "v_lshrrev_b32 v5, 4, v4\n\t"
    "v_and_b32 v7, 8, v4\n\t"
    "v_lshl_add_u32 v7, v7, 3, v5\n\t"
    "v_mul_u32_u24 v22, 14592, v7\n\t"
    "v_lshl_add_u32 v22, v6, 4, v22\n\t"
    "v_bfe_u32 v7, v4, 2, 2\n\t"
    "v_cmp_eq_u32 vcc, v5, v7\n\t"
    "v_and_b32 v7, 1, v4\n\t"
    "v_lshlrev_b32 v7, 4, v7\n\t"
    "v_mov_b32 v8, 0x3f80\n\t"
    "v_lshlrev_b32 v8, v7, v8\n\t"
    "v_cndmask_b32 v8, 0, v8, vcc\n\t"
    "v_and_b32 v7, 2, v4\n\t"
    "v_cmp_eq_u32 vcc, 0, v7\n\t"
    "v_mov_b32 v16, 0\n\t"
    "v_mov_b32 v17, 0\n\t"
    "v_cndmask_b32 v18, 0, v8, vcc\n\t"
    "v_sub_u32 v19, v8, v18\n\t"
    "v_mov_b32 v20, 0\n\t"
    "v_mov_b32 v21, 0\n\t"
    "v_lshrrev_b32 v5, 4, v4\n\t"
    "v_lshlrev_b32 v23, 11, v5\n\t"
    "v_bfe_u32 v5, v4, 2, 2\n\t"
    "v_lshl_add_u32 v23, v5, 6, v23\n\t"
    "v_and_b32 v5, 3, v4\n\t"
    "v_lshl_add_u32 v23, v5, 3, v23\n\t"
    "v_add_u32 v23, %[ldsw], v23\n\t"
    "s_add_u32 s93, s51, 233472\n\t"
    "s_add_u32 s94, s51, 466944\n\t"
    "s_add_u32 s95, s51, 700416\n\t"
    "s_mov_b32 s92, 0\n\t"
    "s_bitcmp1_b32 s46, 5\n\t"
    "s_cbranch_scc1 18f\n\t"
    "s_bitcmp1_b32 s75, 31\n\t"
    "s_cselect_b32 s90, 0x80000000 + " STR1(EPS) " * 23, 0\n\t"
    "s_add_u32 s75, s75, s90\n\t"
    "s_cselect_b32 s89, 0, " STR1(EPS) "\n\t"
    "95:\n\t"
    "s_memtime s[90:91]\n\t"
    "s_waitcnt lgkmcnt(0)\n\t"
    "s_sub_u32 s90, s90, s78\n\t"
    "s_cmp_lt_u32 s90, s75\n\t"
    "s_cbranch_scc1 95b\n\t"
    TP(1)
    "s_memrealtime s[82:83]\n\t"
    "buffer_load_dwordx4 v[0:3], v22, %[rws], s51 offen sc1\n\t"
    "buffer_load_dwordx4 v[4:7], v22, %[rws], s93 offen sc1\n\t"
    "buffer_load_dwordx4 v[8:11], v22, %[rws], s94 offen sc1\n\t"
    "buffer_load_dwordx4 v[12:15], v22, %[rws], s95 offen sc1\n\t"
    "s_waitcnt vmcnt(4)\n\t"
    "ds_read_b64_tr_b16 v[32:33], v23 offset:12288\n\t"
    "ds_read_b64_tr_b16 v[34:35], v23 offset:12544\n\t"
    "ds_read_b64_tr_b16 v[36:37], v23 offset:12800\n\t"
    "ds_read_b64_tr_b16 v[38:39], v23 offset:13056\n\t"
    "ds_read_b64_tr_b16 v[40:41], v23 offset:13312\n\t"
    "ds_read_b64_tr_b16 v[42:43], v23 offset:13568\n\t"
    "ds_read_b64_tr_b16 v[44:45], v23 offset:13824\n\t"
    "ds_read_b64_tr_b16 v[46:47], v23 offset:14080\n\t"
    "ds_read_b64_tr_b16 v[48:49], v23 offset:12320\n\t"
    "ds_read_b64_tr_b16 v[50:51], v23 offset:12576\n\t"
    "ds_read_b64_tr_b16 v[52:53], v23 offset:12832\n\t"
    "ds_read_b64_tr_b16 v[54:55], v23 offset:13088\n\t"
    "ds_read_b64_tr_b16 v[56:57], v23 offset:13344\n\t"
    "ds_read_b64_tr_b16 v[58:59], v23 offset:13600\n\t"
    "ds_read_b64_tr_b16 v[60:61], v23 offset:13856\n\t"
    "ds_read_b64_tr_b16 v[62:63], v23 offset:14112\n\t"
    "s_bitcmp1_b32 s46, 6\n\t"
    "s_cbranch_scc1 24f\n\t"
    "s_cmp_lg_u32 s24, 4\n\t"
    "s_cbranch_scc1 19f\n\t"
    "s_mov_b32 s91, s89\n\t"
    "s_cmp_gt_u32 s81, 5\n\t"
    "s_cbranch_scc1 92f\n\t"
    "s_and_b32 s86, s81, 1\n\t"
    "s_lshl_b32 s86, s86, 6\n\t"
    "s_bitcmp1_b32 %[outw], 11\n\t"
    "s_cselect_b32 s87, 96, 0\n\t"
    "s_lshl4_add_u32 s87, s81, s87\n\t"
    "s_load_dwordx2 s[52:53], %[ka], s86 offset:0\n\t"
    "s_load_dwordx2 s[56:57], %[ka], s86 offset:8\n\t"
    "s_load_dwordx2 s[60:61], %[ka], s86 offset:16\n\t"
    "s_load_dwordx2 s[64:65], %[ka], s86 offset:32\n\t"
    "s_load_dwordx2 s[72:73], %[ka], s87 offset:160\n\t"
    "s_mul_i32 s86, s81, 6\n\t"
    "s_mov_b32 s88, 0xc23d6d74\n\t"
    "s_lshr_b32 s25, s88, s86\n\t"
    "s_lshr_b32 s86, s81, 2\n\t"
    "s_lshl1_add_u32 s86, s86, s81\n\t"
    "s_mul_i32 s86, s86, 1792\n\t"
    "s_add_u32 s86, s86, %[qb]\n\t"
    "s_add_u32 s29, s86, -128\n\t"
    "s_add_u32 s87, s81, 2\n\t"
    "s_lshr_b32 s87, s87, 2\n\t"
    "s_lshl_b32 s86, s87, 25\n\t"
    "s_add_u32 s35, %[kv0], s86\n\t"
    "s_lshl1_add_u32 s87, s87, s81\n\t"
    "s_mul_i32 s87, s87, 1792\n\t"
    "s_add_u32 s86, s87, -128\n\t"
    "s_lshl_b32 s88, s81, 6\n\t"
    "s_sub_u32 s88, s88, 256\n\t"
    "s_and_b32 s89, s81, 5\n\t"
    "s_cselect_b32 s86, s86, s88\n\t"
    "s_add_u32 s30, s86, %[qb]\n\t"
    "s_add_u32 s87, s87, %[mine]\n\t"
    "s_lshl_b32 s86, %[w], 7\n\t"
    "s_lshl1_add_u32 s31, s86, s87\n\t"
    "s_add_u32 s87, s87, s86\n\t"
    "s_add_u32 s32, s87, 1024\n\t"
    "s_mov_b32 s86, -3584\n\t"
    "s_cmp_lt_u32 s81, 2\n\t"
    "s_cselect_b32 s86, 0x7f000000, s86\n\t"
    "s_add_u32 s33, s31, s86\n\t"
    "s_add_u32 s34, s32, s86\n\t"
    "s_min_u32 s86, s81, 1\n\t"
    "s_lshl_b32 s86, s86, 25\n\t"
    "s_add_u32 s36, %[kv0], s86\n\t"
    "s_lshl_b32 s86, s81, 3\n\t"
    "s_and_b32 s87, %[w], 2\n\t"
    "s_lshl1_add_u32 s86, s87, s86\n\t"
    "s_lshr_b32 s86, 0x765342, s86\n\t"
    "s_and_b32 s86, s86, 7\n\t"
    "s_mul_i32 s86, s86, 1792\n\t"
    "s_and_b32 s87, %[w], 1\n\t"
    "s_lshl_b32 s87, s87, 10\n\t"
    "s_add_u32 s86, s86, s87\n\t"
    "s_add_u32 s86, s86, %[mine]\n\t"
    "s_cmp_lt_u32 s81, 3\n\t"
    "s_cselect_b32 s37, s86, 0x7f000000\n\t"
    "92:\n\t"
    "s_mov_b32 s89, s91\n\t"
    "19:\n\t"
    "s_bitcmp1_b32 s46, 4\n\t"
    "s_cbranch_scc1 17f\n\t"
    "2:\n\t"
    "s_waitcnt vmcnt(3)\n\t"
    TP(2)
    "v_mfma_f32_16x16x32_bf16 v[24:27], v[18:21], v[0:3], v[24:27]\n\t"
    "v_max3_u32 v23, v0, v1, v2\n\t"
    "v_mfma_f32_16x16x32_bf16 v[28:31], v[16:19], v[0:3], v[28:31]\n\t"
    "s_waitcnt vmcnt(2)\n\t"
    "v_mfma_f32_16x16x32_bf16 v[24:27], v[18:21], v[4:7], v[24:27]\n\t"
    "v_max3_u32 v23, v23, v3, v4\n\t"
    "v_mfma_f32_16x16x32_bf16 v[28:31], v[16:19], v[4:7], v[28:31]\n\t"
    "v_max3_u32 v23, v23, v5, v6\n\t"
    "s_waitcnt vmcnt(1)\n\t"
    "v_mfma_f32_16x16x32_bf16 v[24:27], v[18:21], v[8:11], v[24:27]\n\t"
    "v_max3_u32 v23, v23, v7, v8\n\t"
    "v_mfma_f32_16x16x32_bf16 v[28:31], v[16:19], v[8:11], v[28:31]\n\t"
    "v_max3_u32 v23, v23, v9, v10\n\t"
    "s_waitcnt vmcnt(0)\n\t"
    "v_mfma_f32_16x16x32_bf16 v[24:27], v[18:21], v[12:15], v[24:27]\n\t"
    "v_max3_u32 v23, v23, v11, v12\n\t"
    "v_mfma_f32_16x16x32_bf16 v[28:31], v[16:19], v[12:15], v[28:31]\n\t"
    "v_max3_u32 v23, v23, v13, v14\n\t"
    "v_max_u32 v23, v23, v15\n\t"
    "v_cmp_eq_u32 vcc, -1, v23\n\t"
    "v_mov_b32 v4, 0x3f803f80\n\t"
    "v_mov_b32 v5, 0x3f803f80\n\t"
    "v_mov_b32 v6, 0x3f803f80\n\t"
    "v_mov_b32 v7, 0x3f803f80\n\t"
    "s_cbranch_vccnz 21f\n\t"
    "3:\n\t"
    "v_add_f32_dpp v24, v24, v24 row_ror:8 row_mask:0xf bank_mask:0xf\n\t"
    "v_add_f32_dpp v25, v25, v25 row_ror:8 row_mask:0xf bank_mask:0xf\n\t"
    "v_add_f32_dpp v26, v26, v26 row_ror:8 row_mask:0xf bank_mask:0xf\n\t"
    "v_add_f32_dpp v27, v27, v27 row_ror:8 row_mask:0xf bank_mask:0xf\n\t"
    "v_add_f32_dpp v28, v28, v28 row_ror:8 row_mask:0xf bank_mask:0xf\n\t"
    "v_add_f32_dpp v29, v29, v29 row_ror:8 row_mask:0xf bank_mask:0xf\n\t"
    "v_add_f32_dpp v30, v30, v30 row_ror:8 row_mask:0xf bank_mask:0xf\n\t"
    "v_add_f32_dpp v31, v31, v31 row_ror:8 row_mask:0xf bank_mask:0xf\n\t"
    "s_not_b64 exec, s[84:85]\n\t"
    "v_cvt_pk_bf16_f32 v4, v24, v25\n\t"
    "v_cvt_pk_bf16_f32 v5, v26, v27\n\t"
    "v_cvt_pk_bf16_f32 v6, v28, v29\n\t"
    "v_cvt_pk_bf16_f32 v7, v30, v31\n\t"
    "s_mov_b64 exec, -1\n\t"
    "s_bitcmp1_b32 s46, 1\n\t"
    "s_cbranch_scc1 16f\n\t"
    "s_waitcnt lgkmcnt(0)\n\t"
    "5:\n\t"
    "v_mfma_f32_16x16x32_bf16 v[8:11], v[4:7], v[4:7], 0\n\t"
    "v_mbcnt_lo_u32_b32 v16, -1, 0\n\t"
    "v_mbcnt_hi_u32_b32 v16, -1, v16\n\t"
    "v_mfma_f32_16x16x32_bf16 v[12:15], v[4:7], v[32:35], 0\n\t"
    "v_mfma_f32_16x16x32_bf16 v[32:35], v[4:7], v[36:39], 0\n\t"
    "v_lshrrev_b32 v17, 4, v16\n\t"
    "v_and_b32 v18, 15, v16\n\t"
    "s_mov_b32 s86, 0x22222222\n\t"
    "v_mfma_f32_16x16x32_bf16 v[36:39], v[4:7], v[40:43], 0\n\t"
    "v_lshlrev_b32 v18, 2, v18\n\t"
    "v_lshl_add_u32 v20, v17, 7, v18\n\t"
    "s_mov_b32 s87, 0\n\t"
    "v_mfma_f32_16x16x32_bf16 v[40:43], v[4:7], v[44:47], 0\n\t"
    "v_add_u32 v20, s48, v20\n\t"
    "v_cndmask_b32_e64 v8, v8, v9, s[86:87]\n\t"
    "s_mov_b32 s86, 0x44444444\n\t"
    "v_mfma_f32_16x16x32_bf16 v[44:47], v[4:7], v[48:51], 0\n\t"
    "v_cndmask_b32_e64 v8, v8, v10, s[86:87]\n\t"
    "s_mov_b32 s86, 0x88888888\n\t"
    "v_cndmask_b32_e64 v8, v8, v11, s[86:87]\n\t"
    "v_mfma_f32_16x16x32_bf16 v[48:51], v[4:7], v[52:55], 0\n\t"
    "v_lshrrev_b32 v19, 3, v16\n\t"
    "v_add_f32_dpp v8, v8, v8 quad_perm:[1,0,3,2] row_mask:0xf bank_mask:0xf\n\t"
    "v_and_b32 v19, 4, v19\n\t"
    "v_mfma_f32_16x16x32_bf16 v[52:55], v[4:7], v[56:59], 0\n\t"
    "v_add_f32_dpp v8, v8, v8 quad_perm:[2,3,0,1] row_mask:0xf bank_mask:0xf\n\t"
    "v_and_b32 v21, 4, v16\n\t"
    "v_lshl_add_u32 v19, v21, 1, v19\n\t"
    "v_mfma_f32_16x16x32_bf16 v[56:59], v[4:7], v[60:63], 0\n\t"
    "v_add_u32 v19, s49, v19\n\t"
    "v_add_f32 v12, v12, v33\n\t"
    "v_add_f32 v38, v38, v43\n\t"
    "s_mov_b32 exec_lo, 0x00100001\n\t"
    "s_mov_b32 exec_hi, 0x00000011\n\t"
    "ds_write_b32 v19, v8\n\t"
    "s_mov_b32 exec_lo, -1\n\t"
    "s_mov_b32 exec_hi, 0x0000ffff\n\t"
    "v_add_f32 v12, v12, v38\n\t"
    "v_add_f32 v44, v44, v49\n\t"
    "v_add_f32 v54, v54, v59\n\t"
    "v_add_f32 v44, v44, v54\n\t"
    "ds_write2_b32 v20, v12, v44 offset1:16\n\t"
    "s_not_b64 exec, s[84:85]\n\t"
    "v_lshrrev_b32 v0, 4, v16\n\t"
    "v_and_b32 v1, 7, v16\n\t"
    "v_lshl_add_u32 v0, v0, 3, v1\n\t"
    "v_lshl_add_u32 v0, v0, 4, s50\n\t"
    "ds_write_b128 v0, v[4:7]\n\t"
    "s_mov_b64 exec, -1\n\t"
    "v_min_u32 v21, 2, v17\n\t"
    "v_lshlrev_b32 v21, 7, v21\n\t"
    "v_lshl_add_u32 v21, v18, 1, v21\n\t"
    "v_add_u32 v21, s47, v21\n\t"
    "v_and_b32 v17, 1, v17\n\t"
    "v_lshl_add_u32 v23, v17, 3, s47\n\t"
    "v_lshlrev_b32 v16, 2, v16\n\t"
    "s_waitcnt lgkmcnt(1)\n\t"
    TP(3)
    "s_barrier\n\t"
    TP(4)
    "s_memtime s[78:79]\n\t"
    "s_cmp_lg_u32 s47, s48\n\t"
    "s_cbranch_scc1 9f\n\t"
    "ds_read_b64 v[24:25], v23 offset:1536\n\t"
    "ds_read_b64 v[26:27], v23 offset:1552\n\t"
    "ds_read_b64 v[28:29], v23 offset:1568\n\t"
    "ds_read_b64 v[30:31], v23 offset:1584\n\t"
    "ds_read_b64 v[8:9], v21 offset:0\n\t"
    "ds_read_b64 v[10:11], v21 offset:384\n\t"
    "ds_read_b64 v[12:13], v21 offset:768\n\t"
    "ds_read_b64 v[14:15], v21 offset:1152\n\t"
    "s_mov_b32 s84, 0x3b000000\n\t"
    "s_waitcnt lgkmcnt(4)\n\t"
    "v_pk_add_f32 v[24:25], v[24:25], v[26:27]\n\t"
    "v_pk_add_f32 v[28:29], v[28:29], v[30:31]\n\t"
    "v_pk_add_f32 v[24:25], v[24:25], v[28:29]\n\t"
    "s_waitcnt lgkmcnt(0)\n\t"
    "v_pk_add_f32 v[8:9], v[8:9], v[10:11]\n\t"
    "v_pk_add_f32 v[12:13], v[12:13], v[14:15]\n\t"
    "v_mul_f32 v25, s84, v25\n\t"
    "v_pk_add_f32 v[8:9], v[8:9], v[12:13]\n\t"
    "v_mul_f32 v26, v25, v25\n\t"
    "v_fma_f32 v24, v24, s84, -v26\n\t"
    "v_permlane32_swap_b32 v8, v10\n\t"
    "v_permlane32_swap_b32 v9, v11\n\t"
    "v_max_f32 v24, 0, v24\n\t"
    "v_add_f32 v24, 0x3727c5ac, v24\n\t"
    "v_rsq_f32 v24, v24\n\t"
    "v_fma_f32 v8, -v25, v10, v8\n\t"
    "v_fma_f32 v9, -v25, v11, v9\n\t"
    "v_mul_f32 v8, v8, v24\n\t"
    "v_mul_f32 v9, v9, v24\n\t"
    "v_cvt_pk_bf16_f32 v8, v8, v9\n\t"
    "s_mov_b32 exec_hi, 0\n\t"
    "v_min_u32 v8, -2, v8\n\t"
    "buffer_store_dword v8, v16, %[rws], s76 offen sc1\n\t"
    "s_mov_b64 exec, -1\n\t"
    TP(5)
    "s_branch 9f\n\t"
    "16:\n\t"
    "v_mbcnt_lo_u32_b32 v16, -1, 0\n\t"
    "v_mbcnt_hi_u32_b32 v16, -1, v16\n\t"
    "v_and_b32 v17, 4, v16\n\t"
    "v_lshlrev_b32 v17, 8, v17\n\t"
    "v_and_b32 v18, 3, v16\n\t"
    "v_lshl_add_u32 v17, v18, 4, v17\n\t"
    "v_and_b32 v18, 48, v16\n\t"
    "v_lshl_add_u32 v17, v18, 2, v17\n\t"
    "s_not_b64 exec, s[84:85]\n\t"
    "buffer_store_dwordx4 v[4:7], v17, s[68:71], s76 offen\n\t"
    "s_mov_b64 exec, -1\n\t"
    "s_waitcnt lgkmcnt(0)\n\t"
    "s_branch 9f\n\t"
    "17:\n\t"
    "s_add_u32 m0, %[ldsw], 8192\n\t"
    "s_add_u32 s90, s51, 3584\n\t"
    "buffer_load_dwordx4 v22, %[rws], s90 offen sc1 lds\n\t"
    "s_add_u32 m0, %[ldsw], 9216\n\t"
    "s_add_u32 s90, s93, 3584\n\t"
    "buffer_load_dwordx4 v22, %[rws], s90 offen sc1 lds\n\t"
    "s_add_u32 m0, %[ldsw], 10240\n\t"
    "s_add_u32 s90, s94, 3584\n\t"
    "buffer_load_dwordx4 v22, %[rws], s90 offen sc1 lds\n\t"
    "s_add_u32 m0, %[ldsw], 11264\n\t"
    "s_add_u32 s90, s95, 3584\n\t"
    "buffer_load_dwordx4 v22, %[rws], s90 offen sc1 lds\n\t"
    "s_branch 2b\n\t"
    "18:\n\t"
    "s_memrealtime s[82:83]\n\t"
    "s_mov_b32 s89, 0\n\t"
    "v_lshl_add_u32 v4, v4, 4, %[ldsw]\n\t"
    "ds_read_b128 v[0:3], v4 offset:8192\n\t"
    "ds_read_b128 v[8:11], v4 offset:10240\n\t"
    "ds_read_b128 v[12:15], v4 offset:11264\n\t"
    "ds_read_b128 v[4:7], v4 offset:9216\n\t"
    "ds_read_b64_tr_b16 v[32:33], v23 offset:12288\n\t"
    "ds_read_b64_tr_b16 v[34:35], v23 offset:12544\n\t"
    "ds_read_b64_tr_b16 v[36:37], v23 offset:12800\n\t"
    "ds_read_b64_tr_b16 v[38:39], v23 offset:13056\n\t"
    "ds_read_b64_tr_b16 v[40:41], v23 offset:13312\n\t"
    "ds_read_b64_tr_b16 v[42:43], v23 offset:13568\n\t"
    "ds_read_b64_tr_b16 v[44:45], v23 offset:13824\n\t"
    "ds_read_b64_tr_b16 v[46:47], v23 offset:14080\n\t"
    "ds_read_b64_tr_b16 v[48:49], v23 offset:12320\n\t"
    "ds_read_b64_tr_b16 v[50:51], v23 offset:12576\n\t"
    "ds_read_b64_tr_b16 v[52:53], v23 offset:12832\n\t"
    "ds_read_b64_tr_b16 v[54:55], v23 offset:13088\n\t"
    "ds_read_b64_tr_b16 v[56:57], v23 offset:13344\n\t"
    "ds_read_b64_tr_b16 v[58:59], v23 offset:13600\n\t"
    "ds_read_b64_tr_b16 v[60:61], v23 offset:13856\n\t"
    "ds_read_b64_tr_b16 v[62:63], v23 offset:14112\n\t"
    "24:\n\t"
    "v_mbcnt_lo_u32_b32 v22, -1, 0\n\t"
    "v_mbcnt_hi_u32_b32 v22, -1, v22\n\t"
    "v_lshrrev_b32 v23, 2, v22\n\t"
    "v_and_b32 v22, 3, v22\n\t"
    "v_lshlrev_b32 v22, 4, v22\n\t"
    "v_lshl_add_u32 v23, v23, 10, v22\n\t"
    "s_waitcnt lgkmcnt(0)\n\t"
    "s_add_u32 m0, %[ldsw], 12288\n\t"
    "s_add_u32 s90, %[wo_off], 0\n\t"
    "buffer_load_dwordx4 v23, s[60:63], s90 offen lds\n\t"
    "s_add_u32 m0, %[ldsw], 13312\n\t"
    "s_add_u32 s90, %[wo_off], 16384\n\t"
    "buffer_load_dwordx4 v23, s[60:63], s90 offen lds\n\t"
    "s_add_u32 m0, %[ldsw], 14336\n\t"
    "s_add_u32 s90, %[wo_off], 32768\n\t"
    "buffer_load_dwordx4 v23, s[60:63], s90 offen lds\n\t"
    "s_add_u32 m0, %[ldsw], 15360\n\t"
    "s_add_u32 s90, %[wo_off], 49152\n\t"
    "buffer_load_dwordx4 v23, s[60:63], s90 offen lds\n\t"
    "s_add_u32 m0, %[ldsw], 16384\n\t"
    "s_add_u32 s90, %[wo_off], 65536\n\t"
    "buffer_load_dwordx4 v23, s[60:63], s90 offen lds\n\t"
    "s_add_u32 m0, %[ldsw], 17408\n\t"
    "s_add_u32 s90, %[wo_off], 81920\n\t"
    "buffer_load_dwordx4 v23, s[60:63], s90 offen lds\n\t"
    "s_add_u32 m0, %[ldsw], 18432\n\t"
    "s_add_u32 s90, %[wo_off], 98304\n\t"
    "buffer_load_dwordx4 v23, s[60:63], s90 offen lds\n\t"
    "s_add_u32 m0, %[ldsw], 19456\n\t"
    "s_add_u32 s90, %[wo_off], 114688\n\t"
    "buffer_load_dwordx4 v23, s[60:63], s90 offen lds\n\t"
    "s_waitcnt vmcnt(8)\n\t"
    "s_branch 22f\n\t"
    "20:\n\t"
    "s_nop 7\n\t"
    "v_mbcnt_lo_u32_b32 v4, -1, 0\n\t"
    "v_mbcnt_hi_u32_b32 v4, -1, v4\n\t"
    "v_lshrrev_b32 v5, 4, v4\n\t"
    "v_and_b32 v6, 7, v4\n\t"
    "v_lshl_add_u32 v5, v5, 3, v6\n\t"
    "v_lshl_add_u32 v5, v5, 4, s50\n\t"
    "ds_read_b128 v[0:3], v5\n\t"
    "s_waitcnt lgkmcnt(0)\n\t"
    "v_cndmask_b32_e64 v0, v0, 0, s[84:85]\n\t"
    "v_cndmask_b32_e64 v1, v1, 0, s[84:85]\n\t"
    "v_cndmask_b32_e64 v2, v2, 0, s[84:85]\n\t"
    "v_cndmask_b32_e64 v3, v3, 0, s[84:85]\n\t"
    "v_lshlrev_b32 v24, 16, v0\n\t"
    "v_and_b32 v25, 0xffff0000, v0\n\t"
    "v_lshlrev_b32 v26, 16, v1\n\t"
    "v_and_b32 v27, 0xffff0000, v1\n\t"
    "v_lshlrev_b32 v28, 16, v2\n\t"
    "v_and_b32 v29, 0xffff0000, v2\n\t"
    "v_lshlrev_b32 v30, 16, v3\n\t"
    "v_and_b32 v31, 0xffff0000, v3\n\t"
    "v_lshrrev_b32 v5, 4, v4\n\t"
    "v_and_b32 v7, 8, v4\n\t"
    "v_lshl_add_u32 v7, v7, 3, v5\n\t"
    "v_mul_u32_u24 v22, 14592, v7\n\t"
    "v_lshl_add_u32 v22, v6, 4, v22\n\t"
    "s_add_u32 s82, s82, s89\n\t"
    "s_mov_b32 s89, 0\n\t"
    "s_waitcnt lgkmcnt(0)\n\t"
    "s_add_u32 s82, s82, " STR1(GTICK) "\n\t"
    "97:\n\t"
    "s_memrealtime s[90:91]\n\t"
    "s_waitcnt lgkmcnt(0)\n\t"
    "s_sub_u32 s90, s90, s82\n\t"
    "s_cmp_lt_i32 s90, 0\n\t"
    "s_cbranch_scc1 97b\n\t"
    "buffer_load_dwordx4 v[0:3], v22, %[rws], s51 offen sc1\n\t"
    "buffer_load_dwordx4 v[4:7], v22, %[rws], s93 offen sc1\n\t"
    "buffer_load_dwordx4 v[8:11], v22, %[rws], s94 offen sc1\n\t"
    "buffer_load_dwordx4 v[12:15], v22, %[rws], s95 offen sc1\n\t"
    "s_branch 19b\n\t"
    "22:\n\t"
    "v_mfma_f32_16x16x32_bf16 v[24:27], v[18:21], v[0:3], v[24:27]\n\t"
    "v_max3_u32 v23, v0, v1, v2\n\t"
    "v_mfma_f32_16x16x32_bf16 v[28:31], v[16:19], v[0:3], v[28:31]\n\t"
    "v_mfma_f32_16x16x32_bf16 v[24:27], v[18:21], v[4:7], v[24:27]\n\t"
    "v_max3_u32 v23, v23, v3, v4\n\t"
    "v_mfma_f32_16x16x32_bf16 v[28:31], v[16:19], v[4:7], v[28:31]\n\t"
    "v_max3_u32 v23, v23, v5, v6\n\t"
    "v_mfma_f32_16x16x32_bf16 v[24:27], v[18:21], v[8:11], v[24:27]\n\t"
    "v_max3_u32 v23, v23, v7, v8\n\t"
    "v_mfma_f32_16x16x32_bf16 v[28:31], v[16:19], v[8:11], v[28:31]\n\t"
    "v_max3_u32 v23, v23, v9, v10\n\t"
    "v_mfma_f32_16x16x32_bf16 v[24:27], v[18:21], v[12:15], v[24:27]\n\t"
    "v_max3_u32 v23, v23, v11, v12\n\t"
    "v_mfma_f32_16x16x32_bf16 v[28:31], v[16:19], v[12:15], v[28:31]\n\t"
    "v_max3_u32 v23, v23, v13, v14\n\t"
    "v_max_u32 v23, v23, v15\n\t"
    "v_cmp_eq_u32 vcc, -1, v23\n\t"
    "v_mov_b32 v4, 0x3f803f80\n\t"
    "v_mov_b32 v5, 0x3f803f80\n\t"
    "v_mov_b32 v6, 0x3f803f80\n\t"
    "v_mov_b32 v7, 0x3f803f80\n\t"
    "s_cbranch_vccz 3b\n\t"
    "21:\n\t"
    "s_add_u32 s92, s92, 1\n\t"
    "s_cmp_gt_u32 s92, 16384\n\t"
    "s_cbranch_scc1 3b\n\t"
    "s_branch 20b\n\t"
    "9:\n\t"
    "s_bitcmp1_b32 s46, 4\n\t"
    "s_cbranch_scc1 .Lgqa_second\n\t"
    ST_STR(5)
    "s_bitcmp1_b32 s46, 1\n\t"
    "s_cbranch_scc0 .Lgqa_step\n\t"
    "s_branch .Lgqa_done\n\t"
    ".Lgqa_second:\n\t"
    "s_mov_b32 s46, 32\n\t"
    "s_mov_b32 s93, 1\n\t"
    "s_lshl2_add_u32 s90, s93, %[w]\n\t"
    "s_lshl_b32 s90, s90, 9\n\t"
    "s_add_u32 s50, s90, 159552\n\t"
    "s_lshl1_add_u32 s90, s93, s24\n\t"
    "s_mul_i32 s90, s90, 1792\n\t"
    "s_add_u32 s51, s90, %[ybase]\n\t"
    "s_add_u32 s90, s90, %[mine]\n\t"
    "s_add_u32 s76, s90, 1664\n\t"
    "s_mov_b32 s47, 155904\n\t"
    "s_mul_i32 s48, %[w], 384\n\t"
    "s_add_u32 s48, s48, s47\n\t"
    "s_lshl4_add_u32 s49, %[w], 157440\n\t"
    "s_branch .Lgqa_s2\n\t"
    ".Lgqa_last:\n\t"
    "s_cmp_gt_u32 %[fin], 1\n\t"
    "s_cbranch_scc1 .Lgqa_done\n\t"
    "s_load_dwordx2 s[68:69], %[ka], 136\n\t"
    "s_mov_b32 s70, 8192\n\t"
    "s_mov_b32 s46, 2\n\t"
    "s_lshl1_add_u32 s24, %[fin], 3\n\t"
    "s_mov_b32 s93, %[fin]\n\t"
    "s_lshl2_add_u32 s90, s93, %[w]\n\t"
    "s_lshl_b32 s90, s90, 9\n\t"
    "s_add_u32 s50, s90, 159552\n\t"
    "s_lshl1_add_u32 s90, s93, s24\n\t"
    "s_mul_i32 s90, s90, 1792\n\t"
    "s_add_u32 s51, s90, %[ybase]\n\t"
    "s_add_u32 s90, s90, %[mine]\n\t"
    "s_add_u32 s76, s90, 1664\n\t"
    "s_lshl_b32 s90, %[fin], 12\n\t"
    "s_add_u32 s76, s90, %[outw]\n\t"
    "s_mov_b32 s24, 5\n\t"
    "s_branch .Lgqa_s2\n\t"
    ".Lgqa_done:\n\t"
    :: [ka] "s"(ka), [rws] "s"(rws), [w] "s"(w), [ldsw] "s"(ldsw), [mine] "s"(mine), [clus] "s"(clus), [qb] "s"(qb), [kv0] "s"(kv0), [wo_off] "s"(wo_off), [wq_off] "s"(wq_off), [ybase] "s"(ybase), [outw] "s"(outw), [fin] "s"(fin), [tp0] "s"(tp0) : "v0", "v1", "v2", "v3", "v4", "v5", "v6", "v7", "v8", "v9", "v10", "v11", "v12", "v13", "v14", "v15", "v16", "v17", "v18", "v19", "v20", "v21", "v22", "v23", "v24", "v25", "v26", "v27", "v28", "v29", "v30", "v31", "v32", "v33", "v34", "v35", "v36", "v37", "v38", "v39", "v40", "v41", "v42", "v43", "v44", "v45", "v46", "v47", "v48", "v49", "v50", "v51", "v52", "v53", "v54", "v55", "v56", "v57", "v58", "v59", "v60", "v61", "v62", "v63", "a0", "a1", "a2", "a3", "a4", "a5", "a6", "a7", "a8", "a9", "a10", "a11", "a12", "a13", "a14", "a15", "a16", "a17", "a18", "a19", "a20", "a21", "a22", "a23", "a24", "a25", "a26", "a27", "a28", "a29", "a30", "a31", "a32", "a33", "a34", "a35", "a36", "a37", "a38", "a39", "a40", "a41", "a42", "a43", "a44", "a45", "a46", "a47", "a48", "a49", "a50", "a51", "a52", "a53", "a54", "a55", "a56", "a57", "a58", "a59", "a60", "a61", "a62", "a63", "s24", "s25", "s26", "s27", "s28", "s29", "s30", "s31", "s32", "s33", "s34", "s35", "s36", "s37", "s38", "s39", "s40", "s41", "s42", "s43", "s44", "s45", "s46", "s47", "s48", "s49", "s50", "s51", "s52", "s53", "s54", "s55", "s56", "s57", "s58", "s59", "s60", "s61", "s62", "s63", "s64", "s65", "s66", "s67", "s68", "s69", "s70", "s71", "s72", "s73", "s74", "s75", "s76", "s77", "s78", "s79", "s80", "s81", "s82", "s83", "s84", "s85", "s86", "s87", "s88", "s89", "s90", "s91", "s92", "s93", "s94", "s95", "s96", "s97", "s98", "s99", "scc", "vcc", "memory");
}

__device__ __forceinline__ void fill_a0(unsigned ldsw, rsrc_t srdk, rsrc_t srdv, unsigned soff) {
  asm volatile(
    "v_mbcnt_lo_u32_b32 v50, -1, 0\n\t"
    "v_mbcnt_hi_u32_b32 v50, -1, v50\n\t"
    "v_lshrrev_b32 v48, 4, v50\n\t"
    "v_xor_b32 v48, v48, v50\n\t"
    "v_and_b32 v48, 3, v48\n\t"
    "v_lshlrev_b32 v45, 6, v48\n\t"
    "v_bfe_u32 v48, v50, 2, 2\n\t"
    "v_lshl_add_u32 v45, v48, 4, v45\n\t"
    "v_and_b32 v48, 15, v50\n\t"
    "v_lshl_add_u32 v45, v48, 8, v45\n\t"
    "v_add_u32 v45, %[ldsw], v45\n\t"
    "v_lshrrev_b32 v48, 4, v50\n\t"
    "v_lshlrev_b32 v44, 2, v48\n\t"
    "v_xor_b32 v44, v44, v50\n\t"
    "v_and_b32 v44, 15, v44\n\t"
    "v_lshlrev_b32 v44, 4, v44\n\t"
    "v_lshl_add_u32 v44, v48, 8, v44\n\t"
    "s_waitcnt vmcnt(8)\n\t"
    "ds_read_b128 a[0:3], v45 offset:0\n\t"
    "v_xor_b32 v48, 16, v45\n\t"
    "ds_read_b128 a[4:7], v48 offset:0\n\t"
    "v_xor_b32 v49, 32, v45\n\t"
    "ds_read_b128 a[8:11], v49 offset:0\n\t"
    "v_xor_b32 v48, 48, v45\n\t"
    "ds_read_b128 a[12:15], v48 offset:0\n\t"
    "s_waitcnt lgkmcnt(0)\n\t"
    "s_add_u32 m0, %[ldsw], 0\n\t"
    "s_add_u32 s90, %[soff], 49152\n\t"
    "buffer_load_dwordx4 v44, %[srdk], s90 offen nt lds\n\t"
    "v_xor_b32 v48, 16, v44\n\t"
    "buffer_load_dwordx4 v48, %[srdk], s90 offen offset:1024 nt lds\n\t"
    "v_xor_b32 v48, 32, v44\n\t"
    "buffer_load_dwordx4 v48, %[srdk], s90 offen offset:2048 nt lds\n\t"
    "v_xor_b32 v48, 48, v44\n\t"
    "buffer_load_dwordx4 v48, %[srdk], s90 offen offset:3072 nt lds\n\t"
    "s_waitcnt vmcnt(8)\n\t"
    "ds_read_b128 a[16:19], v45 offset:4096\n\t"
    "v_xor_b32 v48, 16, v45\n\t"
    "ds_read_b128 a[20:23], v48 offset:4096\n\t"
    "v_xor_b32 v49, 32, v45\n\t"
    "ds_read_b128 a[24:27], v49 offset:4096\n\t"
    "v_xor_b32 v48, 48, v45\n\t"
    "ds_read_b128 a[28:31], v48 offset:4096\n\t"
    "s_waitcnt lgkmcnt(0)\n\t"
    "s_add_u32 m0, %[ldsw], 4096\n\t"
    "s_add_u32 s90, %[soff], 65536\n\t"
    "buffer_load_dwordx4 v44, %[srdk], s90 offen nt lds\n\t"
    "v_xor_b32 v48, 16, v44\n\t"
    "buffer_load_dwordx4 v48, %[srdk], s90 offen offset:1024 nt lds\n\t"
    "v_xor_b32 v48, 32, v44\n\t"
    "buffer_load_dwordx4 v48, %[srdk], s90 offen offset:2048 nt lds\n\t"
    "v_xor_b32 v48, 48, v44\n\t"
    "buffer_load_dwordx4 v48, %[srdk], s90 offen offset:3072 nt lds\n\t"
    "s_waitcnt vmcnt(8)\n\t"
    "ds_read_b128 a[32:35], v45 offset:8192\n\t"
    "v_xor_b32 v48, 16, v45\n\t"
    "ds_read_b128 a[36:39], v48 offset:8192\n\t"
    "v_xor_b32 v49, 32, v45\n\t"
    "ds_read_b128 a[40:43], v49 offset:8192\n\t"
    "v_xor_b32 v48, 48, v45\n\t"
    "ds_read_b128 a[44:47], v48 offset:8192\n\t"
    "s_waitcnt lgkmcnt(0)\n\t"
    "s_add_u32 m0, %[ldsw], 8192\n\t"
    "s_add_u32 s90, %[soff], 81920\n\t"
    "buffer_load_dwordx4 v44, %[srdk], s90 offen nt lds\n\t"
    "v_xor_b32 v48, 16, v44\n\t"
    "buffer_load_dwordx4 v48, %[srdk], s90 offen offset:1024 nt lds\n\t"
    "v_xor_b32 v48, 32, v44\n\t"
    "buffer_load_dwordx4 v48, %[srdk], s90 offen offset:2048 nt lds\n\t"
    "v_xor_b32 v48, 48, v44\n\t"
    "buffer_load_dwordx4 v48, %[srdk], s90 offen offset:3072 nt lds\n\t"
    :: [ldsw] "s"(ldsw), [srdk] "s"(srdk), [srdv] "s"(srdv), [soff] "s"(soff) : "v31", "v32", "v33", "v34", "v35", "v36", "v37", "v38", "v39", "v40", "v41", "v42", "v43", "v44", "v45", "v46", "v47", "v48", "v49", "v50", "v51", "v52", TPCLOB "a0", "a1", "a2", "a3", "a4", "a5", "a6", "a7", "a8", "a9", "a10", "a11", "a12", "a13", "a14", "a15", "a16", "a17", "a18", "a19", "a20", "a21", "a22", "a23", "a24", "a25", "a26", "a27", "a28", "a29", "a30", "a31", "a32", "a33", "a34", "a35", "a36", "a37", "a38", "a39", "a40", "a41", "a42", "a43", "a44", "a45", "a46", "a47", "a48", "a49", "a50", "a51", "a52", "a53", "a54", "a55", "a56", "a57", "a58", "a59", "a60", "a61", "a62", "a63", "s78", "s79", "s80", "s81", "s82", "s83", "s84", "s85", "s86", "s87", "s88", "s89", "s90", "s91", "s92", "s93", "s94", "s95", "scc", "vcc", "memory");
}

__device__ __forceinline__ void fill_a1(unsigned ldsw, rsrc_t srdk, rsrc_t srdv, unsigned soff) {
  asm volatile(
    "v_mbcnt_lo_u32_b32 v50, -1, 0\n\t"
    "v_mbcnt_hi_u32_b32 v50, -1, v50\n\t"
    "v_lshrrev_b32 v48, 4, v50\n\t"
    "v_xor_b32 v48, v48, v50\n\t"
    "v_and_b32 v48, 3, v48\n\t"
    "v_lshlrev_b32 v45, 6, v48\n\t"
    "v_bfe_u32 v48, v50, 2, 2\n\t"
    "v_lshl_add_u32 v45, v48, 4, v45\n\t"
    "v_and_b32 v48, 15, v50\n\t"
    "v_lshl_add_u32 v45, v48, 8, v45\n\t"
    "v_add_u32 v45, %[ldsw], v45\n\t"
    "v_lshrrev_b32 v48, 4, v50\n\t"
    "v_lshlrev_b32 v44, 2, v48\n\t"
    "v_xor_b32 v44, v44, v50\n\t"
    "v_and_b32 v44, 15, v44\n\t"
    "v_lshlrev_b32 v44, 4, v44\n\t"
    "v_lshl_add_u32 v44, v48, 8, v44\n\t"
    "s_waitcnt vmcnt(8)\n\t"
    "ds_read_b128 a[48:51], v45 offset:0\n\t"
    "v_xor_b32 v48, 16, v45\n\t"
    "ds_read_b128 a[52:55], v48 offset:0\n\t"
    "v_xor_b32 v49, 32, v45\n\t"
    "ds_read_b128 a[56:59], v49 offset:0\n\t"
    "v_xor_b32 v48, 48, v45\n\t"
    "ds_read_b128 a[60:63], v48 offset:0\n\t"
    "s_waitcnt lgkmcnt(0)\n\t"
    "s_add_u32 m0, %[ldsw], 0\n\t"
    "s_add_u32 s90, %[soff], 98304\n\t"
    "buffer_load_dwordx4 v44, %[srdk], s90 offen nt lds\n\t"
    "v_xor_b32 v48, 16, v44\n\t"
    "buffer_load_dwordx4 v48, %[srdk], s90 offen offset:1024 nt lds\n\t"
    "v_xor_b32 v48, 32, v44\n\t"
    "buffer_load_dwordx4 v48, %[srdk], s90 offen offset:2048 nt lds\n\t"
    "v_xor_b32 v48, 48, v44\n\t"
    "buffer_load_dwordx4 v48, %[srdk], s90 offen offset:3072 nt lds\n\t"
    "v_lshrrev_b32 v52, 4, v50\n\t"
    "v_lshlrev_b32 v47, 1, v52\n\t"
    "v_xor_b32 v47, v47, v50\n\t"
    "v_and_b32 v47, 15, v47\n\t"
    "v_lshlrev_b32 v47, 4, v47\n\t"
    "v_lshl_add_u32 v47, v52, 8, v47\n\t"
    "s_waitcnt vmcnt(8)\n\t"
    "ds_read_b128 a[64:67], v45 offset:4096\n\t"
    "v_xor_b32 v48, 16, v45\n\t"
    "ds_read_b128 a[68:71], v48 offset:4096\n\t"
    "v_xor_b32 v49, 32, v45\n\t"
    "ds_read_b128 a[72:75], v49 offset:4096\n\t"
    "v_xor_b32 v48, 48, v45\n\t"
    "ds_read_b128 a[76:79], v48 offset:4096\n\t"
    "s_waitcnt lgkmcnt(0)\n\t"
    "s_add_u32 m0, %[ldsw], 4096\n\t"
    "s_add_u32 s90, %[soff], 114688\n\t"
    "buffer_load_dwordx4 v44, %[srdk], s90 offen nt lds\n\t"
    "v_xor_b32 v48, 16, v44\n\t"
    "buffer_load_dwordx4 v48, %[srdk], s90 offen offset:1024 nt lds\n\t"
    "v_xor_b32 v48, 32, v44\n\t"
    "buffer_load_dwordx4 v48, %[srdk], s90 offen offset:2048 nt lds\n\t"
    "v_xor_b32 v48, 48, v44\n\t"
    "buffer_load_dwordx4 v48, %[srdk], s90 offen offset:3072 nt lds\n\t"
    "s_waitcnt vmcnt(8)\n\t"
    "ds_read_b128 a[80:83], v45 offset:8192\n\t"
    "v_xor_b32 v48, 16, v45\n\t"
    "ds_read_b128 a[84:87], v48 offset:8192\n\t"
    "v_xor_b32 v49, 32, v45\n\t"
    "ds_read_b128 a[88:91], v49 offset:8192\n\t"
    "v_xor_b32 v48, 48, v45\n\t"
    "ds_read_b128 a[92:95], v48 offset:8192\n\t"
    "s_waitcnt lgkmcnt(0)\n\t"
    "s_add_u32 m0, %[ldsw], 20480\n\t"
    "s_add_u32 s90, %[soff], 0\n\t"
    "v_xor_b32 v48, 128, v47\n\t"
    "buffer_load_dwordx4 v47, %[srdv], s90 offen offset:0 nt lds\n\t"
    "buffer_load_dwordx4 v48, %[srdv], s90 offen offset:1024 nt lds\n\t"
    "buffer_load_dwordx4 v47, %[srdv], s90 offen offset:2048 nt lds\n\t"
    "buffer_load_dwordx4 v48, %[srdv], s90 offen offset:3072 nt lds\n\t"
    "s_waitcnt vmcnt(8)\n\t"
    "ds_read_b128 a[96:99], v45 offset:0\n\t"
    "v_xor_b32 v48, 16, v45\n\t"
    "ds_read_b128 a[100:103], v48 offset:0\n\t"
    "v_xor_b32 v49, 32, v45\n\t"
    "ds_read_b128 a[104:107], v49 offset:0\n\t"
    "v_xor_b32 v48, 48, v45\n\t"
    "ds_read_b128 a[108:111], v48 offset:0\n\t"
    "s_waitcnt lgkmcnt(0)\n\t"
    "s_add_u32 m0, %[ldsw], 24576\n\t"
    "s_add_u32 s90, %[soff], 16384\n\t"
    "v_xor_b32 v48, 128, v47\n\t"
    "buffer_load_dwordx4 v47, %[srdv], s90 offen offset:0 nt lds\n\t"
    "buffer_load_dwordx4 v48, %[srdv], s90 offen offset:1024 nt lds\n\t"
    "buffer_load_dwordx4 v47, %[srdv], s90 offen offset:2048 nt lds\n\t"
    "buffer_load_dwordx4 v48, %[srdv], s90 offen offset:3072 nt lds\n\t"
    :: [ldsw] "s"(ldsw), [srdk] "s"(srdk), [srdv] "s"(srdv), [soff] "s"(soff) : "v31", "v32", "v33", "v34", "v35", "v36", "v37", "v38", "v39", "v40", "v41", "v42", "v43", "v44", "v45", "v46", "v47", "v48", "v49", "v50", "v51", "v52", TPCLOB "a0", "a1", "a2", "a3", "a4", "a5", "a6", "a7", "a8", "a9", "a10", "a11", "a12", "a13", "a14", "a15", "a16", "a17", "a18", "a19", "a20", "a21", "a22", "a23", "a24", "a25", "a26", "a27", "a28", "a29", "a30", "a31", "a32", "a33", "a34", "a35", "a36", "a37", "a38", "a39", "a40", "a41", "a42", "a43", "a44", "a45", "a46", "a47", "a48", "a49", "a50", "a51", "a52", "a53", "a54", "a55", "a56", "a57", "a58", "a59", "a60", "a61", "a62", "a63", "s78", "s79", "s80", "s81", "s82", "s83", "s84", "s85", "s86", "s87", "s88", "s89", "s90", "s91", "s92", "s93", "s94", "s95", "scc", "vcc", "memory");
}

__device__ __forceinline__ void fill_a2(unsigned ldsw, rsrc_t srdk, rsrc_t srdv, unsigned soff) {
  asm volatile(
    "v_mbcnt_lo_u32_b32 v50, -1, 0\n\t"
    "v_mbcnt_hi_u32_b32 v50, -1, v50\n\t"
    "v_lshrrev_b32 v48, 4, v50\n\t"
    "v_xor_b32 v48, v48, v50\n\t"
    "v_and_b32 v48, 3, v48\n\t"
    "v_lshlrev_b32 v45, 6, v48\n\t"
    "v_bfe_u32 v48, v50, 2, 2\n\t"
    "v_lshl_add_u32 v45, v48, 4, v45\n\t"
    "v_and_b32 v48, 15, v50\n\t"
    "v_lshl_add_u32 v45, v48, 8, v45\n\t"
    "v_add_u32 v45, %[ldsw], v45\n\t"
    "v_lshrrev_b32 v52, 4, v50\n\t"
    "v_lshlrev_b32 v47, 1, v52\n\t"
    "v_xor_b32 v47, v47, v50\n\t"
    "v_and_b32 v47, 15, v47\n\t"
    "v_lshlrev_b32 v47, 4, v47\n\t"
    "v_lshl_add_u32 v47, v52, 8, v47\n\t"
    "s_waitcnt vmcnt(8)\n\t"
    "ds_read_b128 a[112:115], v45 offset:4096\n\t"
    "v_xor_b32 v48, 16, v45\n\t"
    "ds_read_b128 a[116:119], v48 offset:4096\n\t"
    "v_xor_b32 v49, 32, v45\n\t"
    "ds_read_b128 a[120:123], v49 offset:4096\n\t"
    "v_xor_b32 v48, 48, v45\n\t"
    "ds_read_b128 a[124:127], v48 offset:4096\n\t"
    "s_waitcnt lgkmcnt(0)\n\t"
    "s_add_u32 m0, %[ldsw], 0\n\t"
    "s_add_u32 s90, %[soff], 32768\n\t"
    "v_xor_b32 v48, 128, v47\n\t"
    "buffer_load_dwordx4 v47, %[srdv], s90 offen offset:0 nt lds\n\t"
    "buffer_load_dwordx4 v48, %[srdv], s90 offen offset:1024 nt lds\n\t"
    "buffer_load_dwordx4 v47, %[srdv], s90 offen offset:2048 nt lds\n\t"
    "buffer_load_dwordx4 v48, %[srdv], s90 offen offset:3072 nt lds\n\t"
    "s_waitcnt vmcnt(8)\n\t"
    "s_add_u32 m0, %[ldsw], 4096\n\t"
    "s_add_u32 s90, %[soff], 49152\n\t"
    "v_xor_b32 v48, 128, v47\n\t"
    "buffer_load_dwordx4 v47, %[srdv], s90 offen offset:0 nt lds\n\t"
    "buffer_load_dwordx4 v48, %[srdv], s90 offen offset:1024 nt lds\n\t"
    "buffer_load_dwordx4 v47, %[srdv], s90 offen offset:2048 nt lds\n\t"
    "buffer_load_dwordx4 v48, %[srdv], s90 offen offset:3072 nt lds\n\t"
    "s_waitcnt vmcnt(8)\n\t"
    "s_add_u32 m0, %[ldsw], 8192\n\t"
    "s_add_u32 s90, %[soff], 65536\n\t"
    "v_xor_b32 v48, 128, v47\n\t"
    "buffer_load_dwordx4 v47, %[srdv], s90 offen offset:0 nt lds\n\t"
    "buffer_load_dwordx4 v48, %[srdv], s90 offen offset:1024 nt lds\n\t"
    "buffer_load_dwordx4 v47, %[srdv], s90 offen offset:2048 nt lds\n\t"
    "buffer_load_dwordx4 v48, %[srdv], s90 offen offset:3072 nt lds\n\t"
    :: [ldsw] "s"(ldsw), [srdk] "s"(srdk), [srdv] "s"(srdv), [soff] "s"(soff) : "v31", "v32", "v33", "v34", "v35", "v36", "v37", "v38", "v39", "v40", "v41", "v42", "v43", "v44", "v45", "v46", "v47", "v48", "v49", "v50", "v51", "v52", TPCLOB "a0", "a1", "a2", "a3", "a4", "a5", "a6", "a7", "a8", "a9", "a10", "a11", "a12", "a13", "a14", "a15", "a16", "a17", "a18", "a19", "a20", "a21", "a22", "a23", "a24", "a25", "a26", "a27", "a28", "a29", "a30", "a31", "a32", "a33", "a34", "a35", "a36", "a37", "a38", "a39", "a40", "a41", "a42", "a43", "a44", "a45", "a46", "a47", "a48", "a49", "a50", "a51", "a52", "a53", "a54", "a55", "a56", "a57", "a58", "a59", "a60", "a61", "a62", "a63", "s78", "s79", "s80", "s81", "s82", "s83", "s84", "s85", "s86", "s87", "s88", "s89", "s90", "s91", "s92", "s93", "s94", "s95", "scc", "vcc", "memory");
}

__device__ __forceinline__ void first_q(unsigned ldsw, unsigned red, unsigned redw, unsigned stw, unsigned xs, rsrc_t rws, unsigned so_q) {
  asm volatile(
    TP(0)
    "s_mov_b32 s84, 0xff00ff00\n\t"
    "s_mov_b32 s85, 0xff00ff00\n\t"
    "v_mbcnt_lo_u32_b32 v0, -1, 0\n\t"
    "v_mbcnt_hi_u32_b32 v0, -1, v0\n\t"
    "v_lshrrev_b32 v1, 4, v0\n\t"
    "v_and_b32 v2, 7, v0\n\t"
    "v_lshl_add_u32 v1, v1, 3, v2\n\t"
    "v_lshl_add_u32 v1, v1, 4, %[xs]\n\t"
    "ds_read_b128 v[8:11], v1\n\t"
    "v_lshrrev_b32 v2, 4, v0\n\t"
    "v_lshlrev_b32 v23, 11, v2\n\t"
    "v_bfe_u32 v2, v0, 2, 2\n\t"
    "v_lshl_add_u32 v23, v2, 6, v23\n\t"
    "v_and_b32 v2, 3, v0\n\t"
    "v_lshl_add_u32 v23, v2, 3, v23\n\t"
    "v_add_u32 v23, %[ldsw], v23\n\t"
    "ds_read_b64_tr_b16 v[32:33], v23 offset:12288\n\t"
    "ds_read_b64_tr_b16 v[34:35], v23 offset:12544\n\t"
    "ds_read_b64_tr_b16 v[36:37], v23 offset:12800\n\t"
    "ds_read_b64_tr_b16 v[38:39], v23 offset:13056\n\t"
    "ds_read_b64_tr_b16 v[40:41], v23 offset:13312\n\t"
    "ds_read_b64_tr_b16 v[42:43], v23 offset:13568\n\t"
    "ds_read_b64_tr_b16 v[44:45], v23 offset:13824\n\t"
    "ds_read_b64_tr_b16 v[46:47], v23 offset:14080\n\t"
    "ds_read_b64_tr_b16 v[48:49], v23 offset:12320\n\t"
    "ds_read_b64_tr_b16 v[50:51], v23 offset:12576\n\t"
    "ds_read_b64_tr_b16 v[52:53], v23 offset:12832\n\t"
    "ds_read_b64_tr_b16 v[54:55], v23 offset:13088\n\t"
    "ds_read_b64_tr_b16 v[56:57], v23 offset:13344\n\t"
    "ds_read_b64_tr_b16 v[58:59], v23 offset:13600\n\t"
    "ds_read_b64_tr_b16 v[60:61], v23 offset:13856\n\t"
    "ds_read_b64_tr_b16 v[62:63], v23 offset:14112\n\t"
    "v_mov_b32 v12, 0x3f803f80\n\t"
    "s_waitcnt lgkmcnt(0)\n\t"
    "v_cndmask_b32_e64 v4, v8, v12, s[84:85]\n\t"
    "v_cndmask_b32_e64 v5, v9, v12, s[84:85]\n\t"
    "v_cndmask_b32_e64 v6, v10, v12, s[84:85]\n\t"
    "v_cndmask_b32_e64 v7, v11, v12, s[84:85]\n\t"
    "s_nop 1\n\t"
    "5:\n\t"
    "v_mfma_f32_16x16x32_bf16 v[8:11], v[4:7], v[4:7], 0\n\t"
    "v_mbcnt_lo_u32_b32 v16, -1, 0\n\t"
    "v_mbcnt_hi_u32_b32 v16, -1, v16\n\t"
    "v_mfma_f32_16x16x32_bf16 v[12:15], v[4:7], v[32:35], 0\n\t"
    "v_mfma_f32_16x16x32_bf16 v[32:35], v[4:7], v[36:39], 0\n\t"
    "v_lshrrev_b32 v17, 4, v16\n\t"
    "v_and_b32 v18, 15, v16\n\t"
    "s_mov_b32 s86, 0x22222222\n\t"
    "v_mfma_f32_16x16x32_bf16 v[36:39], v[4:7], v[40:43], 0\n\t"
    "v_lshlrev_b32 v18, 2, v18\n\t"
    "v_lshl_add_u32 v20, v17, 7, v18\n\t"
    "s_mov_b32 s87, 0\n\t"
    "v_mfma_f32_16x16x32_bf16 v[40:43], v[4:7], v[44:47], 0\n\t"
    "v_add_u32 v20, %[redw], v20\n\t"
    "v_cndmask_b32_e64 v8, v8, v9, s[86:87]\n\t"
    "s_mov_b32 s86, 0x44444444\n\t"
    "v_mfma_f32_16x16x32_bf16 v[44:47], v[4:7], v[48:51], 0\n\t"
    "v_cndmask_b32_e64 v8, v8, v10, s[86:87]\n\t"
    "s_mov_b32 s86, 0x88888888\n\t"
    "v_cndmask_b32_e64 v8, v8, v11, s[86:87]\n\t"
    "v_mfma_f32_16x16x32_bf16 v[48:51], v[4:7], v[52:55], 0\n\t"
    "v_lshrrev_b32 v19, 3, v16\n\t"
    "v_add_f32_dpp v8, v8, v8 quad_perm:[1,0,3,2] row_mask:0xf bank_mask:0xf\n\t"
    "v_and_b32 v19, 4, v19\n\t"
    "v_mfma_f32_16x16x32_bf16 v[52:55], v[4:7], v[56:59], 0\n\t"
    "v_add_f32_dpp v8, v8, v8 quad_perm:[2,3,0,1] row_mask:0xf bank_mask:0xf\n\t"
    "v_and_b32 v21, 4, v16\n\t"
    "v_lshl_add_u32 v19, v21, 1, v19\n\t"
    "v_mfma_f32_16x16x32_bf16 v[56:59], v[4:7], v[60:63], 0\n\t"
    "v_add_u32 v19, %[stw], v19\n\t"
    "v_add_f32 v12, v12, v33\n\t"
    "v_add_f32 v38, v38, v43\n\t"
    "s_mov_b32 exec_lo, 0x00100001\n\t"
    "s_mov_b32 exec_hi, 0x00000011\n\t"
    "ds_write_b32 v19, v8\n\t"
    "s_mov_b32 exec_lo, -1\n\t"
    "s_mov_b32 exec_hi, 0x0000ffff\n\t"
    "v_add_f32 v12, v12, v38\n\t"
    "v_add_f32 v44, v44, v49\n\t"
    "v_add_f32 v54, v54, v59\n\t"
    "v_add_f32 v44, v44, v54\n\t"
    "ds_write2_b32 v20, v12, v44 offset1:16\n\t"
    "s_not_b64 exec, s[84:85]\n\t"
    "v_lshrrev_b32 v0, 4, v16\n\t"
    "v_and_b32 v1, 7, v16\n\t"
    "v_lshl_add_u32 v0, v0, 3, v1\n\t"
    "v_lshl_add_u32 v0, v0, 4, %[xs]\n\t"
    "ds_write_b128 v0, v[4:7]\n\t"
    "s_mov_b64 exec, -1\n\t"
    "v_min_u32 v21, 2, v17\n\t"
    "v_lshlrev_b32 v21, 7, v21\n\t"
    "v_lshl_add_u32 v21, v18, 1, v21\n\t"
    "v_add_u32 v21, %[red], v21\n\t"
    "v_and_b32 v17, 1, v17\n\t"
    "v_lshl_add_u32 v23, v17, 3, %[red]\n\t"
    "v_lshlrev_b32 v16, 2, v16\n\t"
    "s_waitcnt lgkmcnt(1)\n\t"
    TP(3)
    "s_barrier\n\t"
    TP(4)
    "s_memtime s[78:79]\n\t"
    "s_cmp_lg_u32 %[red], %[redw]\n\t"
    "s_cbranch_scc1 9f\n\t"
    "ds_read_b64 v[24:25], v23 offset:1536\n\t"
    "ds_read_b64 v[26:27], v23 offset:1552\n\t"
    "ds_read_b64 v[28:29], v23 offset:1568\n\t"
    "ds_read_b64 v[30:31], v23 offset:1584\n\t"
    "ds_read_b64 v[8:9], v21 offset:0\n\t"
    "ds_read_b64 v[10:11], v21 offset:384\n\t"
    "ds_read_b64 v[12:13], v21 offset:768\n\t"
    "ds_read_b64 v[14:15], v21 offset:1152\n\t"
    "s_mov_b32 s84, 0x3b000000\n\t"
    "s_waitcnt lgkmcnt(4)\n\t"
    "v_pk_add_f32 v[24:25], v[24:25], v[26:27]\n\t"
    "v_pk_add_f32 v[28:29], v[28:29], v[30:31]\n\t"
    "v_pk_add_f32 v[24:25], v[24:25], v[28:29]\n\t"
    "s_waitcnt lgkmcnt(0)\n\t"
    "v_pk_add_f32 v[8:9], v[8:9], v[10:11]\n\t"
    "v_pk_add_f32 v[12:13], v[12:13], v[14:15]\n\t"
    "v_mul_f32 v25, s84, v25\n\t"
    "v_pk_add_f32 v[8:9], v[8:9], v[12:13]\n\t"
    "v_mul_f32 v26, v25, v25\n\t"
    "v_fma_f32 v24, v24, s84, -v26\n\t"
    "v_permlane32_swap_b32 v8, v10\n\t"
    "v_permlane32_swap_b32 v9, v11\n\t"
    "v_max_f32 v24, 0, v24\n\t"
    "v_add_f32 v24, 0x3727c5ac, v24\n\t"
    "v_rsq_f32 v24, v24\n\t"
    "v_fma_f32 v8, -v25, v10, v8\n\t"
    "v_fma_f32 v9, -v25, v11, v9\n\t"
    "v_mul_f32 v8, v8, v24\n\t"
    "v_mul_f32 v9, v9, v24\n\t"
    "v_cvt_pk_bf16_f32 v8, v8, v9\n\t"
    "s_mov_b32 exec_hi, 0\n\t"
    "v_min_u32 v8, -2, v8\n\t"
    "buffer_store_dword v8, v16, %[rws], %[so_q] offen sc1\n\t"
    "s_mov_b64 exec, -1\n\t"
    TP(5)
    "s_branch 9f\n\t"
    "9:\n\t"
    :: [ldsw] "s"(ldsw), [red] "s"(red), [redw] "s"(redw), [stw] "s"(stw), [xs] "s"(xs), [rws] "s"(rws), [so_q] "s"(so_q) : "v0", "v1", "v2", "v3", "v4", "v5", "v6", "v7", "v8", "v9", "v10", "v11", "v12", "v13", "v14", "v15", "v16", "v17", "v18", "v19", "v20", "v21", "v22", "v23", "v24", "v25", "v26", "v27", "v28", "v29", "v30", "v31", "v32", "v33", "v34", "v35", "v36", "v37", "v38", "v39", "v40", "v41", "v42", "v43", "v44", "v45", "v46", "v47", "v48", "v49", "v50", "v51", "v52", "v53", "v54", "v55", "v56", "v57", "v58", "v59", "v60", "v61", "v62", "v63", TPCLOB "a0", "a1", "a2", "a3", "a4", "a5", "a6", "a7", "a8", "a9", "a10", "a11", "a12", "a13", "a14", "a15", "a16", "a17", "a18", "a19", "a20", "a21", "a22", "a23", "a24", "a25", "a26", "a27", "a28", "a29", "a30", "a31", "a32", "a33", "a34", "a35", "a36", "a37", "a38", "a39", "a40", "a41", "a42", "a43", "a44", "a45", "a46", "a47", "a48", "a49", "a50", "a51", "a52", "a53", "a54", "a55", "a56", "a57", "a58", "a59", "a60", "a61", "a62", "a63", "s78", "s79", "s80", "s81", "s82", "s83", "s84", "s85", "s86", "s87", "s88", "s89", "s90", "s91", "s92", "s93", "s94", "s95", "scc", "vcc", "memory");
}

__device__ __forceinline__ void wdma_q(unsigned ldsw, rsrc_t srdw, unsigned woff) {
  asm volatile(
    "v_mbcnt_lo_u32_b32 v31, -1, 0\n\t"
    "v_mbcnt_hi_u32_b32 v31, -1, v31\n\t"
    "v_lshrrev_b32 v30, 2, v31\n\t"
    "v_and_b32 v31, 3, v31\n\t"
    "v_lshlrev_b32 v31, 4, v31\n\t"
    "v_lshl_add_u32 v30, v30, 13, v31\n\t"
    "s_add_u32 m0, %[ldsw], 12288\n\t"
    "s_add_u32 s90, %[woff], 0\n\t"
    "buffer_load_dwordx4 v30, %[srdw], s90 offen lds\n\t"
    "s_add_u32 m0, %[ldsw], 13312\n\t"
    "s_add_u32 s90, %[woff], 131072\n\t"
    "buffer_load_dwordx4 v30, %[srdw], s90 offen lds\n\t"
    "s_add_u32 m0, %[ldsw], 14336\n\t"
    "s_add_u32 s90, %[woff], 262144\n\t"
    "buffer_load_dwordx4 v30, %[srdw], s90 offen lds\n\t"
    "s_add_u32 m0, %[ldsw], 15360\n\t"
    "s_add_u32 s90, %[woff], 393216\n\t"
    "buffer_load_dwordx4 v30, %[srdw], s90 offen lds\n\t"
    "s_add_u32 m0, %[ldsw], 16384\n\t"
    "s_add_u32 s90, %[woff], 524288\n\t"
    "buffer_load_dwordx4 v30, %[srdw], s90 offen lds\n\t"
    "s_add_u32 m0, %[ldsw], 17408\n\t"
    "s_add_u32 s90, %[woff], 655360\n\t"
    "buffer_load_dwordx4 v30, %[srdw], s90 offen lds\n\t"
    "s_add_u32 m0, %[ldsw], 18432\n\t"
    "s_add_u32 s90, %[woff], 786432\n\t"
    "buffer_load_dwordx4 v30, %[srdw], s90 offen lds\n\t"
    "s_add_u32 m0, %[ldsw], 19456\n\t"
    "s_add_u32 s90, %[woff], 917504\n\t"
    "buffer_load_dwordx4 v30, %[srdw], s90 offen lds\n\t"
    :: [ldsw] "s"(ldsw), [srdw] "s"(srdw), [woff] "s"(woff) : "v30", "v31", TPCLOB "a0", "a1", "a2", "a3", "a4", "a5", "a6", "a7", "a8", "a9", "a10", "a11", "a12", "a13", "a14", "a15", "a16", "a17", "a18", "a19", "a20", "a21", "a22", "a23", "a24", "a25", "a26", "a27", "a28", "a29", "a30", "a31", "a32", "a33", "a34", "a35", "a36", "a37", "a38", "a39", "a40", "a41", "a42", "a43", "a44", "a45", "a46", "a47", "a48", "a49", "a50", "a51", "a52", "a53", "a54", "a55", "a56", "a57", "a58", "a59", "a60", "a61", "a62", "a63", "s78", "s79", "s80", "s81", "s82", "s83", "s84", "s85", "s86", "s87", "s88", "s89", "s90", "s91", "s92", "s93", "s94", "s95", "scc", "vcc", "memory");
}

__device__ __forceinline__ void wdma_o(unsigned ldsw, rsrc_t srdw, unsigned woff) {
  asm volatile(
    "v_mbcnt_lo_u32_b32 v31, -1, 0\n\t"
    "v_mbcnt_hi_u32_b32 v31, -1, v31\n\t"
    "v_lshrrev_b32 v30, 2, v31\n\t"
    "v_and_b32 v31, 3, v31\n\t"
    "v_lshlrev_b32 v31, 4, v31\n\t"
    "v_lshl_add_u32 v30, v30, 10, v31\n\t"
    "s_add_u32 m0, %[ldsw], 12288\n\t"
    "s_add_u32 s90, %[woff], 0\n\t"
    "buffer_load_dwordx4 v30, %[srdw], s90 offen lds\n\t"
    "s_add_u32 m0, %[ldsw], 13312\n\t"
    "s_add_u32 s90, %[woff], 16384\n\t"
    "buffer_load_dwordx4 v30, %[srdw], s90 offen lds\n\t"
    "s_add_u32 m0, %[ldsw], 14336\n\t"
    "s_add_u32 s90, %[woff], 32768\n\t"
    "buffer_load_dwordx4 v30, %[srdw], s90 offen lds\n\t"
    "s_add_u32 m0, %[ldsw], 15360\n\t"
    "s_add_u32 s90, %[woff], 49152\n\t"
    "buffer_load_dwordx4 v30, %[srdw], s90 offen lds\n\t"
    "s_add_u32 m0, %[ldsw], 16384\n\t"
    "s_add_u32 s90, %[woff], 65536\n\t"
    "buffer_load_dwordx4 v30, %[srdw], s90 offen lds\n\t"
    "s_add_u32 m0, %[ldsw], 17408\n\t"
    "s_add_u32 s90, %[woff], 81920\n\t"
    "buffer_load_dwordx4 v30, %[srdw], s90 offen lds\n\t"
    "s_add_u32 m0, %[ldsw], 18432\n\t"
    "s_add_u32 s90, %[woff], 98304\n\t"
    "buffer_load_dwordx4 v30, %[srdw], s90 offen lds\n\t"
    "s_add_u32 m0, %[ldsw], 19456\n\t"
    "s_add_u32 s90, %[woff], 114688\n\t"
    "buffer_load_dwordx4 v30, %[srdw], s90 offen lds\n\t"
    :: [ldsw] "s"(ldsw), [srdw] "s"(srdw), [woff] "s"(woff) : "v30", "v31", TPCLOB "a0", "a1", "a2", "a3", "a4", "a5", "a6", "a7", "a8", "a9", "a10", "a11", "a12", "a13", "a14", "a15", "a16", "a17", "a18", "a19", "a20", "a21", "a22", "a23", "a24", "a25", "a26", "a27", "a28", "a29", "a30", "a31", "a32", "a33", "a34", "a35", "a36", "a37", "a38", "a39", "a40", "a41", "a42", "a43", "a44", "a45", "a46", "a47", "a48", "a49", "a50", "a51", "a52", "a53", "a54", "a55", "a56", "a57", "a58", "a59", "a60", "a61", "a62", "a63", "s78", "s79", "s80", "s81", "s82", "s83", "s84", "s85", "s86", "s87", "s88", "s89", "s90", "s91", "s92", "s93", "s94", "s95", "scc", "vcc", "memory");
}

__device__ __forceinline__ void pre3(unsigned ldsw, rsrc_t srdk, unsigned soff) {
  asm volatile(
    "v_mbcnt_lo_u32_b32 v29, -1, 0\n\t"
    "v_mbcnt_hi_u32_b32 v29, -1, v29\n\t"
    "v_lshrrev_b32 v31, 4, v29\n\t"
    "v_lshlrev_b32 v30, 2, v31\n\t"
    "v_xor_b32 v30, v30, v29\n\t"
    "v_and_b32 v30, 15, v30\n\t"
    "v_lshlrev_b32 v30, 4, v30\n\t"
    "v_lshl_add_u32 v30, v31, 8, v30\n\t"
    "s_add_u32 m0, %[ldsw], 0\n\t"
    "s_add_u32 s90, %[soff], 0\n\t"
    "buffer_load_dwordx4 v30, %[srdk], s90 offen nt lds\n\t"
    "v_xor_b32 v31, 16, v30\n\t"
    "buffer_load_dwordx4 v31, %[srdk], s90 offen offset:1024 nt lds\n\t"
    "v_xor_b32 v31, 32, v30\n\t"
    "buffer_load_dwordx4 v31, %[srdk], s90 offen offset:2048 nt lds\n\t"
    "v_xor_b32 v31, 48, v30\n\t"
    "buffer_load_dwordx4 v31, %[srdk], s90 offen offset:3072 nt lds\n\t"
    "s_add_u32 m0, %[ldsw], 4096\n\t"
    "s_add_u32 s90, %[soff], 16384\n\t"
    "buffer_load_dwordx4 v30, %[srdk], s90 offen nt lds\n\t"
    "v_xor_b32 v31, 16, v30\n\t"
    "buffer_load_dwordx4 v31, %[srdk], s90 offen offset:1024 nt lds\n\t"
    "v_xor_b32 v31, 32, v30\n\t"
    "buffer_load_dwordx4 v31, %[srdk], s90 offen offset:2048 nt lds\n\t"
    "v_xor_b32 v31, 48, v30\n\t"
    "buffer_load_dwordx4 v31, %[srdk], s90 offen offset:3072 nt lds\n\t"
    "s_add_u32 m0, %[ldsw], 8192\n\t"
    "s_add_u32 s90, %[soff], 32768\n\t"
    "buffer_load_dwordx4 v30, %[srdk], s90 offen nt lds\n\t"
    "v_xor_b32 v31, 16, v30\n\t"
    "buffer_load_dwordx4 v31, %[srdk], s90 offen offset:1024 nt lds\n\t"
    "v_xor_b32 v31, 32, v30\n\t"
    "buffer_load_dwordx4 v31, %[srdk], s90 offen offset:2048 nt lds\n\t"
    "v_xor_b32 v31, 48, v30\n\t"
    "buffer_load_dwordx4 v31, %[srdk], s90 offen offset:3072 nt lds\n\t"
    :: [ldsw] "s"(ldsw), [srdk] "s"(srdk), [soff] "s"(soff) : "v29", "v30", "v31", TPCLOB "a0", "a1", "a2", "a3", "a4", "a5", "a6", "a7", "a8", "a9", "a10", "a11", "a12", "a13", "a14", "a15", "a16", "a17", "a18", "a19", "a20", "a21", "a22", "a23", "a24", "a25", "a26", "a27", "a28", "a29", "a30", "a31", "a32", "a33", "a34", "a35", "a36", "a37", "a38", "a39", "a40", "a41", "a42", "a43", "a44", "a45", "a46", "a47", "a48", "a49", "a50", "a51", "a52", "a53", "a54", "a55", "a56", "a57", "a58", "a59", "a60", "a61", "a62", "a63", "s78", "s79", "s80", "s81", "s82", "s83", "s84", "s85", "s86", "s87", "s88", "s89", "s90", "s91", "s92", "s93", "s94", "s95", "scc", "vcc", "memory");
}

__device__ __forceinline__ void pre3x(unsigned ldsw, rsrc_t srdk, unsigned soff, unsigned skip) {
  asm volatile(
    "s_cmp_lg_u32 %[skip], 0\n\t"
    "s_cbranch_scc1 1f\n\t"
    "v_mbcnt_lo_u32_b32 v29, -1, 0\n\t"
    "v_mbcnt_hi_u32_b32 v29, -1, v29\n\t"
    "v_lshrrev_b32 v31, 4, v29\n\t"
    "v_lshlrev_b32 v30, 2, v31\n\t"
    "v_xor_b32 v30, v30, v29\n\t"
    "v_and_b32 v30, 15, v30\n\t"
    "v_lshlrev_b32 v30, 4, v30\n\t"
    "v_lshl_add_u32 v30, v31, 8, v30\n\t"
    "s_add_u32 m0, %[ldsw], 0\n\t"
    "s_add_u32 s90, %[soff], 0\n\t"
    "buffer_load_dwordx4 v30, %[srdk], s90 offen nt lds\n\t"
    "v_xor_b32 v31, 16, v30\n\t"
    "buffer_load_dwordx4 v31, %[srdk], s90 offen offset:1024 nt lds\n\t"
    "v_xor_b32 v31, 32, v30\n\t"
    "buffer_load_dwordx4 v31, %[srdk], s90 offen offset:2048 nt lds\n\t"
    "v_xor_b32 v31, 48, v30\n\t"
    "buffer_load_dwordx4 v31, %[srdk], s90 offen offset:3072 nt lds\n\t"
    "s_add_u32 m0, %[ldsw], 4096\n\t"
    "s_add_u32 s90, %[soff], 16384\n\t"
    "buffer_load_dwordx4 v30, %[srdk], s90 offen nt lds\n\t"
    "v_xor_b32 v31, 16, v30\n\t"
    "buffer_load_dwordx4 v31, %[srdk], s90 offen offset:1024 nt lds\n\t"
    "v_xor_b32 v31, 32, v30\n\t"
    "buffer_load_dwordx4 v31, %[srdk], s90 offen offset:2048 nt lds\n\t"
    "v_xor_b32 v31, 48, v30\n\t"
    "buffer_load_dwordx4 v31, %[srdk], s90 offen offset:3072 nt lds\n\t"
    "s_add_u32 m0, %[ldsw], 8192\n\t"
    "s_add_u32 s90, %[soff], 32768\n\t"
    "buffer_load_dwordx4 v30, %[srdk], s90 offen nt lds\n\t"
    "v_xor_b32 v31, 16, v30\n\t"
    "buffer_load_dwordx4 v31, %[srdk], s90 offen offset:1024 nt lds\n\t"
    "v_xor_b32 v31, 32, v30\n\t"
    "buffer_load_dwordx4 v31, %[srdk], s90 offen offset:2048 nt lds\n\t"
    "v_xor_b32 v31, 48, v30\n\t"
    "buffer_load_dwordx4 v31, %[srdk], s90 offen offset:3072 nt lds\n\t"
    "1:\n\t"
    :: [ldsw] "s"(ldsw), [srdk] "s"(srdk), [soff] "s"(soff), [skip] "s"(skip) : "v29", "v30", "v31", TPCLOB "a0", "a1", "a2", "a3", "a4", "a5", "a6", "a7", "a8", "a9", "a10", "a11", "a12", "a13", "a14", "a15", "a16", "a17", "a18", "a19", "a20", "a21", "a22", "a23", "a24", "a25", "a26", "a27", "a28", "a29", "a30", "a31", "a32", "a33", "a34", "a35", "a36", "a37", "a38", "a39", "a40", "a41", "a42", "a43", "a44", "a45", "a46", "a47", "a48", "a49", "a50", "a51", "a52", "a53", "a54", "a55", "a56", "a57", "a58", "a59", "a60", "a61", "a62", "a63", "s78", "s79", "s80", "s81", "s82", "s83", "s84", "s85", "s86", "s87", "s88", "s89", "s90", "s91", "s92", "s93", "s94", "s95", "scc", "vcc", "memory");
}

__device__ __forceinline__ unsigned lane_id() {
  unsigned l;
  asm volatile("v_mbcnt_lo_u32_b32 %0, -1, 0\n\tv_mbcnt_hi_u32_b32 %0, -1, %0" : "=v"(l));
  return l;
}
__device__ __forceinline__ void wg_barrier() { asm volatile("s_waitcnt lgkmcnt(0)\n\ts_barrier" ::: "memory"); }
__device__ __forceinline__ rsrc_t mk(const void* p, unsigned bytes) { return __builtin_amdgcn_make_buffer_rsrc((void*)p, 0, (int)bytes, 0x00020000); }
__device__ __forceinline__ void store_tag(rsrc_t rws, unsigned off, unsigned long long tag, unsigned t) {
  asm volatile("v_mov_b32 v0, %2\n\tv_mov_b32 v1, %3\n\tv_mov_b32 v2, %4\n\ts_mov_b64 exec, 1\n\tbuffer_store_dwordx3 v[0:2], off, %0, %1 sc1\n\ts_mov_b64 exec, -1\n\ts_nop 1"
               :: "s"(rws), "s"(off), "s"((unsigned)tag), "s"((unsigned)(tag >> 32)), "s"(t) : "v0", "v1", "v2", "memory");
}
__device__ __forceinline__ unsigned long long next_tag(unsigned lo, unsigned hi) {
  const unsigned long long e = ((unsigned long long)(unsigned)__builtin_amdgcn_readfirstlane(hi) << 32) | (unsigned)__builtin_amdgcn_readfirstlane(lo);
  return e * 6364136223846793005ull + 1442695040888963407ull;
}
__device__ __forceinline__ void warm8(unsigned ldsw, rsrc_t r, unsigned off, unsigned step) {
  asm volatile("v_mbcnt_lo_u32_b32 v0, -1, 0\n\tv_mbcnt_hi_u32_b32 v0, -1, v0\n\tv_lshlrev_b32 v0, 4, v0\n\ts_mov_b32 m0, %0\n\ts_mov_b32 s90, %2\n\t"
               ".rept 8\n\tbuffer_load_dwordx4 v0, %1, s90 offen lds\n\ts_add_u32 s90, s90, %3\n\t.endr"
               :: "s"(ldsw), "s"(r), "s"(off), "s"(step) : "v0", "s90", "scc", "memory");
}
#define OPQ(x) asm volatile("" : "+s"(x))

struct Mat { const void *k, *v, *wo, *wq_next, *k_next, *unused[3]; };
struct Args {
  Mat mat[2];
  const void* x;
  void* out;
  void* ws;
  unsigned xmin, pad0;
  unsigned sched[2][6][4];
  unsigned x0, xy, y0, pad1;
};
static_assert(sizeof(Args) == 368 && offsetof(Args, out) == 136 && offsetof(Args, xmin) == 152 && offsetof(Args, sched) == 160, "argument layout");

extern "C" __global__ void __launch_bounds__(256, 1) __attribute__((amdgpu_num_vgpr(64))) gqa_chain(Args a) {
  extern __shared__ __attribute__((aligned(16))) char smem[];
  asm volatile("; reserve the whole register file" ::: "v255", "a255");
  asm volatile("s_getpc_b64 s[84:85]\n.Lgqa_code_begin:\n\ts_mov_b32 s86, .Lgqa_code_end - .Lgqa_code_begin\n\ts_mov_b32 s87, 0x20000\n\t"
               "s_lshr_b32 s88, %1, 3\n\tv_lshrrev_b32 v1, 4, %0\n\tv_lshl_add_u32 v1, s88, 4, v1\n\tv_lshlrev_b32 v1, 6, v1\n\ts_mov_b32 m0, 28672\n\t"
               "s_mov_b32 exec_lo, 0x00010001\n\ts_mov_b32 exec_hi, 0x00010001\n\tbuffer_load_dword v1, s[84:87], 0 offen lds\n\ts_mov_b64 exec, -1"
               :: "v"(threadIdx.x), "s"(blockIdx.x) : "v1", "s84", "s85", "s86", "s87", "s88", "scc", "memory");
  unsigned ka0, ka1;
  asm volatile("s_load_dword %0, %2, 0xc0\n\ts_load_dword %1, %2, 0x100" : "=&s"(ka0), "=&s"(ka1) : "s"(__builtin_amdgcn_kernarg_segment_ptr()));
  __builtin_amdgcn_sched_barrier(0);
  const unsigned lds0 = (unsigned)(size_t)(LAS char*)smem;
  const unsigned w = __builtin_amdgcn_readfirstlane(threadIdx.x >> 6);
  const unsigned bid = blockIdx.x, hkv = bid & 7, j = bid >> 3, set = j & 1, jj = j >> 1, bl = jj >> 3, sp = jj & 7;
  const unsigned ldsw = lds0 + w * LDS_WAVE;
  const rsrc_t rws = mk(a.ws, WS_END + ((STAMPS || TUNE) ? 256 * 256 + 4096 : 0));
  const unsigned cu = (set * 8 + hkv) * 16 + jj;
  const unsigned clus = WS_SLOT + (set * 8 + hkv) * 16 * CU_SZ;
  const unsigned mine = WS_SLOT + cu * CU_SZ;
  const unsigned wo_off = (hkv * 512 + w * 128) * 1024 + jj * 64;
  const unsigned wq_off = (w * 128) * 8192 + hkv * 1024 + jj * 64;
  const unsigned kv0 = (((set * 2 + bl) * 8 + hkv) * 4096 + sp * 512 + w * 16) * 256;
  const void *pk0 = a.mat[0].k, *pv0 = a.mat[0].v, *po0 = a.mat[0].wo, *pq1 = a.mat[0].wq_next, *po1 = a.mat[1].wo, *pq0 = a.mat[1].wq_next;
  unsigned long long tst;
  asm volatile("s_memrealtime %0" : "=s"(tst), "+s"(pk0), "+s"(po1), "+s"(pq0));
  pre3x(ldsw, mk(pk0, 64u << 20), kv0, set);
  asm volatile("s_waitcnt lgkmcnt(0)" : "+s"(tst), "+s"(ka0), "+s"(ka1));
  STAMP(0);

  unsigned tpoll;
  unsigned long long tag;
  {
    const unsigned tstart = (unsigned)tst;
    const rsrc_t rk = mk(pk0, 64u << 20), rv = mk(pv0, 64u << 20), rx = mk(a.x, 8192);
    if (set == 1) {
      const unsigned l = lane_id(), n = l & 15, g = l >> 4;
      const unsigned other = mine - 128 * CU_SZ;
      v4u e0, x0, x1;
      const unsigned xoff = (((n >> 3) * 4 + ((n >> 2) & 1)) * 512 + w * 128 + g * 32 + (n & 3) * 8) * 2;
      asm volatile("buffer_load_dwordx4 %0, off, %1, 0 sc1" : "=&v"(e0) : "s"(rws) : "memory");
      {
        const v4u sent = {SENT, SENT, SENT, SENT};
        const unsigned vo = w * 1024 + l * 16 < 256 + 2 * SLOT_SZ ? l * 16 : 0x7fff0000u;
        __builtin_amdgcn_raw_buffer_store_b128(sent, rws, vo, other + O_Q0 + w * 1024, AUX_SC1);
        __builtin_amdgcn_raw_buffer_store_b128(sent, rws, vo, mine + O_Q0 + w * 1024, AUX_SC1);
      }
      asm volatile("buffer_load_dwordx4 %0, %2, %3, 0 offen\n\tbuffer_load_dwordx4 %1, %2, %3, 0 offen offset:2048"
                   : "=&v"(x0), "=&v"(x1) : "v"(xoff), "s"(rx) : "memory");
      const unsigned o_o = hkv * 524288 + (jj * 4 + w) * 8192, o_q = (jj * 4 + w) * 8 * 8192 + hkv * 1024;
      warm8(ldsw, mk(pq0, 4u << 20), o_q, 8192u);
      asm volatile("s_waitcnt vmcnt(8)" : "+v"(e0), "+v"(x0), "+v"(x1)::"memory");
      const unsigned xl = ((n >> 3) * 4 + w) * 512 + (g * 8 + (n & 7)) * 16;
      *(LAS v4u*)(size_t)(lds0 + REC + xl) = x0;
      *(LAS v4u*)(size_t)(lds0 + XS + xl) = x1;
      const unsigned long long t0 = next_tag(e0.x, e0.y);
      tag = next_tag(e0.z, e0.w);
      wg_barrier();
      STAMP(4);
      if (w == 1) {
        store_tag(rws, WS_RDY + (cu - 128) * 128, t0, tstart);
        store_tag(rws, WS_RDY + cu * 128, tag, tstart);
      }
      asm volatile("s_waitcnt vmcnt(2)\n\ts_barrier" ::: "memory");
      wdma_q(ldsw, mk(pq0, 4u << 20), wq_off);
#pragma unroll 1
      for (unsigned k = 0; k < 4; ++k) {
        OPQ(k);
        if (k < 3) warm8(ldsw, mk(k == 0 ? po0 : k == 1 ? pq1 : po1, 4u << 20), k == 1 ? o_q : o_o, k == 1 ? 8192u : 1024u);
        asm volatile("s_waitcnt vmcnt(8)" ::: "memory");
        const unsigned s2 = k & 1, gam = k >> 1;
        const unsigned red = lds0 + R2 - s2 * 2048;
        first_q(ldsw, red, red + w * 384, red + 1536 + w * 16, lds0 + (s2 ? XS : REC) + (gam * 4 + w) * 512, rws, (s2 ? mine : other) + O_Q0 + gam * 128);
      }
      wdma_o(ldsw, mk(po0, 4u << 20), wo_off);
      asm volatile("s_waitcnt vmcnt(8)" ::: "memory");
      STAMP(1);
    }
    const unsigned lf = lane_id();
    const unsigned vo = WS_RDY + (set * 128 + (lf < 32 ? w * 32 + lf : 0)) * 128;
    v4u f0;
    if (set == 1) {
      asm volatile("buffer_load_dwordx4 %0, %1, %2, 0 offen sc1" : "=&v"(f0) : "v"(vo), "s"(rws) : "memory");
      while ((int)((unsigned)__builtin_amdgcn_s_memrealtime() - (tstart + a.y0)) < 0) { }
      wg_barrier();
      pre3(ldsw, rk, kv0);
      fill_a0(ldsw, rk, rv, kv0);
      asm volatile("" : "+v"(f0));
    } else {
      v2u ev; v4u x0;
      const unsigned l = lane_id(), n = l & 15, g = l >> 4;
      const unsigned xoff = (((n >> 3) * 4 + ((n >> 2) & 1)) * 512 + w * 128 + g * 32 + (n & 3) * 8) * 2;
      asm volatile("buffer_load_dwordx2 %0, off, %2, 0 sc1\n\tbuffer_load_dwordx4 %1, %3, %4, 0 offen"
                   : "=&v"(ev), "=&v"(x0) : "s"(rws), "v"(xoff), "s"(rx) : "memory");
      fill_a0(ldsw, rk, rv, kv0);
      fill_a1(ldsw, rk, rv, kv0);
      asm volatile("buffer_load_dwordx4 %0, %3, %4, 0 offen sc1" : "=&v"(f0), "+v"(ev), "+v"(x0) : "v"(vo), "s"(rws) : "memory");
      tag = next_tag(ev.x, ev.y);
      *(LAS v4u*)(size_t)(lds0 + XS + ((n >> 3) * 4 + w) * 512 + (g * 8 + (n & 7)) * 16) = x0;
      fill_a2(ldsw, rk, rv, kv0);
      asm volatile("" : "+v"(f0));
    }
    STAMP(2);
    for (int spin = 0; !__all(f0.x == (unsigned)tag && f0.y == (unsigned)(tag >> 32)) && spin <= SPIN_CAP; ++spin)
      asm volatile("s_sleep 4\n\tbuffer_load_dwordx4 %0, %1, %2, 0 offen sc1\n\ts_waitcnt vmcnt(0)" : "=&v"(f0) : "v"(vo), "s"(rws) : "memory");
    {
      unsigned tref = __builtin_amdgcn_readlane(f0.z, 63);
      if (tref - tstart + 300u > 600u) tref = tstart;
      tpoll = tref + (set ? a.xy : a.x0);
    }
    wg_barrier();
    if (hkv == 1 && jj == 0 && w == 0) *(LAS unsigned long long*)(size_t)(lds0 + TAGW) = tag;
    STAMP(3);
  }

  main_loop((unsigned long long)__builtin_amdgcn_kernarg_segment_ptr(), rws, w, ldsw, mine, clus, clus + bl * 64, kv0, wo_off, wq_off,
            WS_SLOT + (set * 128 + w * 4) * CU_SZ + O_Y, set * 2048 + w * 256, hkv == 0 ? jj : 99u, tpoll);
  unsigned b2 = bid, w2 = w;
  OPQ(b2); OPQ(w2);
  if ((b2 & ~8u) == 1 && w2 == 0) {
    const v2u t = *(LAS v2u*)(size_t)(lds0 + TAGW);
    if (lane_id() == 0) __builtin_amdgcn_raw_buffer_store_b64(t, rws, 0, WS_E + (b2 & 8), AUX_SC1);
  }
  asm volatile(".Lgqa_code_end:");
}

extern "C" void kernel_launch(void* const* d_in, const int* in_sizes, int n_in, void* d_out, int out_size, void* d_ws, size_t ws_size,
                              hipStream_t stream) {
  Args a;
  a.x = d_in[0];
  a.mat[0].k = d_in[1]; a.mat[0].v = d_in[2]; a.mat[0].wo = d_in[4]; a.mat[0].wq_next = d_in[8];
  a.mat[1].k = d_in[6]; a.mat[1].v = d_in[7]; a.mat[1].wo = d_in[9]; a.mat[1].wq_next = d_in[3];
  a.mat[0].k_next = d_in[6]; a.mat[1].k_next = d_in[1];
  for (int i = 0; i < 3; ++i) a.mat[0].unused[i] = a.mat[1].unused[i] = nullptr;
  a.x0 = g_tune[0]; a.xy = g_tune[1]; a.y0 = g_tune[2]; a.xmin = g_tune[6]; a.pad0 = a.pad1 = 0;
  for (int t = 0; t < 2; ++t)
    for (int s = 0; s < 6; ++s) {
      const bool quiet1 = t ? s >= 3 : s >= 6, quiet3 = t ? s >= 4 : s >= 6;
      a.sched[t][s][0] = (s >= 1 && s <= 3) ? g_tune[(t ? 12 : 2) + s] : 0;
      a.sched[t][s][2] = s >= 4 ? g_tune[10] + (t ? 0 : g_tune[16]) : (quiet1 ? g_tune[17] : g_tune[7]);
      a.sched[t][s][3] = g_tune[quiet1 ? 11 : 8];
      a.sched[t][s][1] = s == 3 ? 0 : g_tune[quiet3 ? 12 : 9];
    }
  a.out = d_out;
  a.ws = d_ws;
  (void)hipFuncSetAttribute((const void*)gqa_chain, hipFuncAttributeMaxDynamicSharedMemorySize, (int)LDS_TOTAL);
  hipLaunchKernelGGL(gqa_chain, dim3(256), dim3(256), LDS_TOTAL, stream, a);
}
```
